```python
import torch

def _softmax(x, axis=-1):
    return torch.softmax(x, dim=axis)
def _gelu(x):
    return torch.nn.functional.gelu(x, approximate="tanh")
def _silu(x):
    return torch.nn.functional.silu(x)
def _sigmoid(x):
    return torch.sigmoid(x)
def _layernorm(x, axis=-1, eps=1e-5):
    m = x.mean(dim=axis, keepdim=True)
    v = x.var(dim=axis, keepdim=True, unbiased=False)
    return (x - m) / torch.sqrt(v + eps)
def _scatter_add(tgt, idx, src, axis=0):
    return tgt.clone().index_add_(axis, idx, src)
def _segment_sum(src, seg, n):
    out = torch.zeros((n,) + src.shape[1:], dtype=src.dtype)
    return out.index_add_(0, seg, src)
def _pad(x, pw):
    flat = tuple(v for pair in reversed(pw) for v in pair)
    return torch.nn.functional.pad(x, flat)

kernel_name = 'standalone__sliding_block_cached__sampling'

def setup_inputs():

    _w = (lambda a: (lambda t: t.to(torch.bfloat16) if t.is_floating_point() else t)(torch.as_tensor(a)))
    B, Sq, D = 16, 1, 576
    Hq, Hkv, Sk, Dh = 32, 16, 4096, 128
    assert Hq % Hkv == 0, (Hq, Hkv)
    return {
        "x": _w((lambda *s: torch.randn(*s, dtype=torch.bfloat16) * 0.8)(B, Sq, D)),
        "k_cache": _w((lambda *s: torch.randn(*s, dtype=torch.bfloat16) * 0.8)(B, Hkv, Sk, Dh)),
        "v_cache": _w((lambda *s: torch.randn(*s, dtype=torch.bfloat16) * 0.05)(B, Hkv, Sk, Dh)),
        "wq": _w((lambda *s: torch.randn(*s, dtype=torch.bfloat16) * 0.8)(D, Hq * Dh) * (D ** -0.5)),
        "wo": _w((lambda *s: torch.randn(*s, dtype=torch.bfloat16) * 0.05)(Hq * Dh, D) * ((Hq * Dh) ** -0.5)),
    }

def reference(x, k_cache, v_cache, wq, wo):
    B, Sq, D = x.shape
    Hq, Dh = 32, 128
    Hkv, Sk = k_cache.shape[1], k_cache.shape[2]
    W = min(4096, max(256, Sk // 4))
    rep = Hq // Hkv
    q = (lambda x, *d: x.permute(*d))((x @ wq).reshape(B, Sq, Hq, Dh), 0, 2, 1, 3)
    k = (lambda x, n, axis: torch.repeat_interleave(x, n, dim=axis))(k_cache[:, :, Sk - W:, :], rep, axis=1)
    v = (lambda x, n, axis: torch.repeat_interleave(x, n, dim=axis))(v_cache[:, :, Sk - W:, :], rep, axis=1)
    scores = torch.einsum("bhqd,bhkd->bhqk", q, k) * (Dh ** -0.5)
    p = _softmax(scores, axis=-1)
    o = torch.einsum("bhqk,bhkd->bhqd", p, v)
    return (lambda x, *d: x.permute(*d))(o, 0, 2, 1, 3).reshape(B, Sq, Hq * Dh) @ wo
```

```cpp
#include <hip/hip_runtime.h>
#include <hip/hip_bf16.h>
#include <stdint.h>

#define GAS __attribute__((address_space(1)))
#define LAS __attribute__((address_space(3)))
typedef unsigned u32x4 __attribute__((ext_vector_type(4)));
typedef unsigned u32x2 __attribute__((ext_vector_type(2)));
typedef float f32x4 __attribute__((ext_vector_type(4)));
typedef float f32x2 __attribute__((ext_vector_type(2)));
typedef short s16x8 __attribute__((ext_vector_type(8)));
typedef short s16x4 __attribute__((ext_vector_type(4)));
typedef __bf16 bf16x8 __attribute__((ext_vector_type(8)));
typedef __bf16 bf16x2 __attribute__((ext_vector_type(2)));

namespace {

constexpr int NB = 16, DM = 576, HKV = 16, SK = 4096, DH = 128, WIN = 1024, NQ = 4096;
constexpr int NW = 16, NT = NW * 64;
constexpr int NL = 256 / NW;
constexpr int NV0 = NL / 2;
constexpr int KSQ = DM / 32;

#define KV_AUX 2
constexpr int AUX_SC1 = 16;
constexpr int RSRC_W3 = 0x00020000;

constexpr unsigned WS_EPOCH = 0;
constexpr unsigned WS_QG = 4096;
constexpr unsigned WS_OG = WS_QG + 256 * 1024;
constexpr unsigned WS_PG = WS_OG + 256 * 1024;
constexpr unsigned WS_BYTES = WS_PG + 36 * 16 * 2 * 1024;

constexpr int L_WQ = 0;
constexpr int L_X = L_WQ + 18432;
constexpr int L_WO = L_X + 18432;
constexpr int L_RED = L_WO + 32768;
constexpr int L_OA = L_RED + NW * 1024;
constexpr int L_Q = L_OA + 16 * 528;
constexpr int L_ML = L_Q + 512;
constexpr int L_TAG = L_ML + NW * 16;
constexpr int L_BYTES = L_TAG + 16;

constexpr unsigned SPIN_CAP = 1u << 18;

__device__ __forceinline__ float dot2(unsigned k, unsigned q, float acc) {
  return __builtin_amdgcn_fdot2_f32_bf16(__builtin_bit_cast(bf16x2, k), __builtin_bit_cast(bf16x2, q), acc, false);
}
__device__ __forceinline__ unsigned pkbf(float lo, float hi) {
  f32x2 f = {lo, hi};
  return __builtin_bit_cast(unsigned, __builtin_convertvector(f, bf16x2));
}
template <int CTRL> __device__ __forceinline__ float dpp(float x) {
  return __builtin_bit_cast(float, __builtin_amdgcn_mov_dpp(__builtin_bit_cast(int, x), CTRL, 0xf, 0xf, true));
}
constexpr int XOR1 = 0xB1, XOR2 = 0x4E, HMIRROR = 0x141, ROR8 = 0x128;
__device__ __forceinline__ float xrow_max(float x) {
  auto s = __builtin_amdgcn_permlane16_swap(__float_as_uint(x), __float_as_uint(x), false, false);
  x = fmaxf(__uint_as_float(s[0]), __uint_as_float(s[1]));
  auto t = __builtin_amdgcn_permlane32_swap(__float_as_uint(x), __float_as_uint(x), false, false);
  return fmaxf(__uint_as_float(t[0]), __uint_as_float(t[1]));
}
__device__ __forceinline__ float xrow_sum(float x) {
  auto s = __builtin_amdgcn_permlane16_swap(__float_as_uint(x), __float_as_uint(x), false, false);
  x = __uint_as_float(s[0]) + __uint_as_float(s[1]);
  auto t = __builtin_amdgcn_permlane32_swap(__float_as_uint(x), __float_as_uint(x), false, false);
  return __uint_as_float(t[0]) + __uint_as_float(t[1]);
}
__device__ __forceinline__ f32x4 mfma16(u32x4 a, u32x4 b, f32x4 c) {
  return __builtin_amdgcn_mfma_f32_16x16x32_bf16(__builtin_bit_cast(bf16x8, a), __builtin_bit_cast(bf16x8, b), c, 0, 0, 0);
}
__device__ __forceinline__ void dma16(unsigned lds, unsigned voff, __amdgpu_buffer_rsrc_t rs, unsigned soff) {
  asm volatile("s_mov_b32 m0, %0\n\ts_nop 0\n\tbuffer_load_dwordx4 %1, %2, %3 offen lds"
               :: "s"(lds), "v"(voff), "s"(rs), "s"(soff) : "memory");
}
__device__ __forceinline__ u32x2 trread(LAS unsigned char* p) {
  s16x4 v = __builtin_amdgcn_ds_read_tr16_b64_v4i16((LAS s16x4*)p);
  return __builtin_bit_cast(u32x2, v);
}

__global__ void __launch_bounds__(NT, NW / 4)
fused_decode_kernel(const unsigned char* __restrict__ x, const unsigned char* __restrict__ kc,
                    const unsigned char* __restrict__ vc, const unsigned char* __restrict__ wq,
                    const unsigned char* __restrict__ wo, unsigned char* __restrict__ out,
                    unsigned char* ws) {
  __shared__ __attribute__((aligned(16))) unsigned char smem_[L_BYTES];
  LAS unsigned char* smem = (LAS unsigned char*)smem_;
  const unsigned lds0 = (unsigned)(size_t)smem;
  const int tid = threadIdx.x, lane = tid & 63;
  const int wave = __builtin_amdgcn_readfirstlane(tid >> 6);
  const int bid = blockIdx.x, jj = bid >> 3, kvh = 2 * (bid & 7) + (jj >> 4), b = jj & 15;
  const int li = lane & 15, lg = lane >> 4;

  const __amdgpu_buffer_rsrc_t rs_ws = __builtin_amdgcn_make_buffer_rsrc(ws, 0, WS_BYTES, RSRC_W3);
  const size_t kvoff = ((size_t)(b * HKV + kvh) * SK + (SK - WIN)) * (DH * 2);
  const __amdgpu_buffer_rsrc_t rs_k = __builtin_amdgcn_make_buffer_rsrc((void*)(kc + kvoff), 0, WIN * DH * 2, RSRC_W3);
  const __amdgpu_buffer_rsrc_t rs_v = __builtin_amdgcn_make_buffer_rsrc((void*)(vc + kvoff), 0, WIN * DH * 2, RSRC_W3);

  unsigned epoch = __builtin_amdgcn_raw_buffer_load_b32(rs_ws, WS_EPOCH, 0, 0);
  __builtin_amdgcn_sched_barrier(0);
  if (wave == 0) {
    const __amdgpu_buffer_rsrc_t rs_wq = __builtin_amdgcn_make_buffer_rsrc((void*)wq, 0, DM * NQ * 2, RSRC_W3);
    const __amdgpu_buffer_rsrc_t rs_x = __builtin_amdgcn_make_buffer_rsrc((void*)x, 0, NB * DM * 2, RSRC_W3);
    const unsigned vwq = (unsigned)(lane >> 1) * (NQ * 2) + (unsigned)(lane & 1) * 16 + (unsigned)(kvh * 256 + 16 * b) * 2;
    const unsigned vx = (unsigned)li * (DM * 2) + (unsigned)lg * 16;
#pragma unroll 1
    for (int s = 0; s < KSQ; ++s) {
      dma16(lds0 + L_WQ + s * 1024, vwq, rs_wq, (unsigned)s * (32 * NQ * 2));
      dma16(lds0 + L_X + s * 1024, vx, rs_x, (unsigned)s * 64);
    }
  } else if (wave >= NW - 8 && b < 9) {
    const __amdgpu_buffer_rsrc_t rs_wo = __builtin_amdgcn_make_buffer_rsrc((void*)wo, 0, NQ * DM * 2, RSRC_W3);
    const unsigned vwo = (unsigned)(lane >> 3) * (DM * 2) + (unsigned)(lane & 7) * 16;
    const int i0 = (wave - (NW - 8)) * 4;
#pragma unroll 1
    for (int i = i0; i < i0 + 4; ++i)
      dma16(lds0 + L_WO + i * 1024, vwo, rs_wo, (unsigned)(kvh * 256 + 8 * i) * (DM * 2) + (unsigned)b * 128);
  }
  __builtin_amdgcn_sched_barrier(0);

  const unsigned voff = (unsigned)tid * 16;
  u32x4 K[NL], V[NL];
#pragma unroll
  for (int u = 0; u < NL; ++u) {
    K[u] = __builtin_amdgcn_raw_buffer_load_b128(rs_k, voff, u * (NW * 1024), KV_AUX);
    __builtin_amdgcn_sched_barrier(0);
  }

  if (wave == 0) {
    asm volatile("s_waitcnt vmcnt(%0)" :: "i"(NL) : "memory");
    unsigned e0 = epoch;
    asm volatile("" : "+v"(e0));
    const unsigned tag = e0 + 1u;
    f32x4 acc = {0.f, 0.f, 0.f, 0.f};
    LAS unsigned char* pw = smem + L_WQ + (8 * lg + ((lane >> 2) & 3)) * 32 + (lane & 3) * 8;
    LAS unsigned char* px = smem + L_X + lane * 16;
#pragma unroll 6
    for (int s = 0; s < KSQ; ++s) {
      const u32x2 w0 = trread(pw + s * 1024), w1 = trread(pw + s * 1024 + 128);
      const u32x4 xb = *(LAS u32x4*)(px + s * 1024);
      acc = mfma16(u32x4{w0[0], w0[1], w1[0], w1[1]}, xb, acc);
    }
    const u32x4 g = {pkbf(acc[0], acc[1]), tag, pkbf(acc[2], acc[3]), tag};
    __builtin_amdgcn_raw_buffer_store_b128(g, rs_ws, WS_QG + (unsigned)(kvh * 16 + li) * 1024 + (unsigned)(16 * b + 4 * lg) * 4, 0, AUX_SC1);
    u32x4 r;
    for (unsigned spins = 0;; ++spins) {
      r = __builtin_amdgcn_raw_buffer_load_b128(rs_ws, WS_QG + (unsigned)(kvh * 16 + b) * 1024 + (unsigned)lane * 16, 0, AUX_SC1);
      if (__all(r[1] == tag && r[3] == tag) || spins > SPIN_CAP) break;
      __builtin_amdgcn_s_sleep(2);
      asm volatile("" ::: "memory");
    }
    *(LAS u32x2*)(smem + L_Q + lane * 8) = u32x2{r[0], r[2]};
  }
  __builtin_amdgcn_sched_barrier(0);
#pragma unroll
  for (int u = 0; u < NV0; ++u) V[u] = __builtin_amdgcn_raw_buffer_load_b128(rs_v, voff, u * (NW * 1024), KV_AUX);
  __builtin_amdgcn_sched_barrier(0);
  __syncthreads();

  const int hA = li >> 3;
  const u32x4 qA = *(LAS u32x4*)(smem + L_Q + hA * 256 + li * 16);
  const u32x4 qB = *(LAS u32x4*)(smem + L_Q + (hA ^ 1) * 256 + li * 16);
  float y[NL];
#pragma unroll
  for (int u = 0; u < NL; ++u) {
    float xa = 0.f, xb = 0.f;
#pragma unroll
    for (int k = 0; k < 4; ++k) { xa = dot2(K[u][k], qA[k], xa); xb = dot2(K[u][k], qB[k], xb); }
    float t = xa + dpp<ROR8>(xb);
    t += dpp<XOR1>(t);
    t += dpp<XOR2>(t);
    t += dpp<HMIRROR>(t);
    y[u] = t;
    if (u == NL - NV0 - 1) {
      __builtin_amdgcn_sched_barrier(0);
#pragma unroll
      for (int v = NV0; v < NL; ++v) V[v] = __builtin_amdgcn_raw_buffer_load_b128(rs_v, voff, v * (NW * 1024), KV_AUX);
      __builtin_amdgcn_sched_barrier(0);
    }
  }
  asm volatile("" : "+v"(epoch));
  const unsigned tag = epoch + 1u;
  constexpr float C2 = 0.088388347648318440f * 1.4426950408889634f;
  float m = y[0];
#pragma unroll
  for (int u = 1; u < NL; ++u) m = fmaxf(m, y[u]);
  m = xrow_max(m);
  const float mc = m * C2;
  float l = 0.f;
#pragma unroll
  for (int u = 0; u < NL; ++u) { y[u] = __builtin_amdgcn_exp2f(fmaf(y[u], C2, -mc)); l += y[u]; }
  l = xrow_sum(l);
  if ((lane & 55) == 0) *(LAS f32x2*)(smem + L_ML + hA * (NW * 8) + wave * 8) = f32x2{mc, l};
  __syncthreads();
  {
    f32x4 ml[NW / 2];
#pragma unroll
    for (int w = 0; w < NW / 2; ++w) ml[w] = *(LAS f32x4*)(smem + L_ML + hA * (NW * 8) + w * 16);
    float M = fmaxf(ml[0][0], ml[0][2]);
#pragma unroll
    for (int w = 1; w < NW / 2; ++w) M = fmaxf(M, fmaxf(ml[w][0], ml[w][2]));
    float L = 0.f;
#pragma unroll
    for (int w = 0; w < NW / 2; ++w) {
      L = fmaf(ml[w][1], __builtin_amdgcn_exp2f(ml[w][0] - M), L);
      L = fmaf(ml[w][3], __builtin_amdgcn_exp2f(ml[w][2] - M), L);
    }
    const float f = __builtin_amdgcn_exp2f(mc - M) / L;
#pragma unroll
    for (int u = 0; u < NL; ++u) y[u] *= f;
  }
  const unsigned hm = (lane & 1) ? 0xffff0000u : 0x0000ffffu;
  const int rsel = (lane & 7) >> 1;
  const unsigned mk0 = rsel == 0 ? hm : 0u, mk1 = rsel == 1 ? hm : 0u, mk2 = rsel == 2 ? hm : 0u, mk3 = rsel == 3 ? hm : 0u;
  f32x4 acc = {0.f, 0.f, 0.f, 0.f};
#pragma unroll
  for (int u = 0; u < NL; ++u) {
    const unsigned pp = pkbf(y[u], y[u]);
    acc = mfma16(V[u], u32x4{pp & mk0, pp & mk1, pp & mk2, pp & mk3}, acc);
  }
  *(LAS f32x4*)(smem + L_RED + wave * 1024 + lane * 16) = acc;
  __syncthreads();
  if (wave == 0) {
    f32x4 pr[NW];
#pragma unroll
    for (int w = 0; w < NW; ++w) pr[w] = *(LAS f32x4*)(smem + L_RED + w * 1024 + lane * 16);
    __builtin_amdgcn_sched_barrier(0);
#pragma unroll
    for (int st = 1; st < NW; st *= 2)
#pragma unroll
      for (int w = 0; w < NW; w += 2 * st) pr[w] += pr[w + st];
    const f32x4 o = pr[0];
    const u32x4 g = {pkbf(o[0], o[1]), tag, pkbf(o[2], o[3]), tag};
    __builtin_amdgcn_raw_buffer_store_b128(g, rs_ws, WS_OG + (unsigned)(kvh * 16 + b) * 1024 + (unsigned)lane * 16, 0, AUX_SC1);
  }
  if (b < 9) {
    u32x4 wf[8];
    const int w4 = wave & 3;
    if (wave >= 4 && wave < 8) {
      LAS unsigned char* pw = smem + L_WO + (8 * ((lane >> 2) & 3) + 2 * lg) * 128 + w4 * 32 + (lane & 3) * 8;
#pragma unroll
      for (int s = 0; s < 8; ++s) {
        const int ro = ((s & 1) * 128 + 32 * (s >> 1)) * 128;
        const u32x2 w0 = trread(pw + ro), w1 = trread(pw + ro + 128);
        wf[s] = u32x4{w0[0], w0[1], w1[0], w1[1]};
      }
    }
#pragma unroll 1
    for (int pm = wave; pm < 16; pm += NW) {
      u32x4 r;
      for (unsigned spins = 0;; ++spins) {
        r = __builtin_amdgcn_raw_buffer_load_b128(rs_ws, WS_OG + (unsigned)(kvh * 16 + pm) * 1024 + (unsigned)lane * 16, 0, AUX_SC1);
        if (__all(r[1] == tag && r[3] == tag) || spins > SPIN_CAP) break;
        __builtin_amdgcn_s_sleep(1);
        asm volatile("" ::: "memory");
      }
      *(LAS u32x2*)(smem + L_OA + pm * 528 + lane * 8) = u32x2{r[0], r[2]};
    }
    __syncthreads();
    if (wave >= 4 && wave < 8) {
      f32x4 d = {0.f, 0.f, 0.f, 0.f};
#pragma unroll
      for (int s = 0; s < 8; ++s) {
        const u32x4 ob = *(LAS u32x4*)(smem + L_OA + li * 528 + s * 64 + lg * 16);
        d = mfma16(wf[s], ob, d);
      }
      const unsigned po = WS_PG + (unsigned)(((4 * b + w4) * 16 + kvh) * 2) * 1024 + (unsigned)lane * 16;
      __builtin_amdgcn_raw_buffer_store_b128(u32x4{__float_as_uint(d[0]), tag, __float_as_uint(d[1]), tag}, rs_ws, po, 0, AUX_SC1);
      __builtin_amdgcn_raw_buffer_store_b128(u32x4{__float_as_uint(d[2]), tag, __float_as_uint(d[3]), tag}, rs_ws, po + 1024, 0, AUX_SC1);
    }
  } else if (wave == 1) {
    const int rho = (b - 9) * 16 + kvh;
    if (rho < 72) {
      const int T = rho >> 1, hf = rho & 1;
      const unsigned po = WS_PG + (unsigned)(T * 32 + hf) * 1024 + (unsigned)lane * 16;
      u32x4 r[16];
      for (unsigned spins = 0;; ++spins) {
        bool ok = true;
#pragma unroll
        for (int h = 0; h < 16; ++h) r[h] = __builtin_amdgcn_raw_buffer_load_b128(rs_ws, po, h * 2048, AUX_SC1);
#pragma unroll
        for (int h = 0; h < 16; ++h) ok = ok && r[h][1] == tag && r[h][3] == tag;
        if (__all(ok) || spins > SPIN_CAP) break;
        __builtin_amdgcn_s_sleep(1);
        asm volatile("" ::: "memory");
      }
      float s0 = 0.f, s1 = 0.f;
#pragma unroll
      for (int h = 0; h < 16; ++h) { s0 += __uint_as_float(r[h][0]); s1 += __uint_as_float(r[h][2]); }
      *(GAS unsigned*)(out + (size_t)(li * DM + 16 * T + 4 * lg + 2 * hf) * 2) = pkbf(s0, s1);
      if (rho == 0 && lane == 0) __builtin_amdgcn_raw_buffer_store_b32(tag, rs_ws, WS_EPOCH, 0, AUX_SC1);
    }
  }
}

}

extern "C" void kernel_launch(void* const* d_in, const int* in_sizes, int n_in, void* d_out, int out_size,
                              void* d_ws, size_t ws_size, hipStream_t stream) {
  fused_decode_kernel<<<256, NT, 0, stream>>>((const unsigned char*)d_in[0], (const unsigned char*)d_in[1],
                                             (const unsigned char*)d_in[2], (const unsigned char*)d_in[3],
                                             (const unsigned char*)d_in[4], (unsigned char*)d_out,
                                             (unsigned char*)d_ws);
}
```

```cpp
#include <hip/hip_runtime.h>
#include <hip/hip_bf16.h>
#include <stdint.h>

#define GAS __attribute__((address_space(1)))
#define LAS __attribute__((address_space(3)))
typedef unsigned u32x4 __attribute__((ext_vector_type(4)));
typedef unsigned u32x2 __attribute__((ext_vector_type(2)));
typedef float f32x4 __attribute__((ext_vector_type(4)));
typedef float f32x2 __attribute__((ext_vector_type(2)));
typedef short s16x8 __attribute__((ext_vector_type(8)));
typedef short s16x4 __attribute__((ext_vector_type(4)));
typedef __bf16 bf16x8 __attribute__((ext_vector_type(8)));
typedef __bf16 bf16x2 __attribute__((ext_vector_type(2)));

namespace {

constexpr int NB = 16, DM = 576, HKV = 16, SK = 4096, DH = 128, WIN = 1024, NQ = 4096;
#define NWAVES 16
constexpr int NW = NWAVES, NT = NW * 64;
constexpr int NL = 256 / NW;
#define KWIN 2
#ifndef NVPRE
#define NVPRE 2
#endif
#define QPS 4
#ifndef NVX
#define NVX 2
#endif
#define X_NT 0
#define VWIN 2
constexpr int KSQ = DM / 32;

#define KV_AUX 2
constexpr int AUX_SC1 = 16;
constexpr int RSRC_W3 = 0x00020000;

constexpr unsigned WS_EPOCH = 0;
constexpr unsigned WS_QG = 4096;
constexpr unsigned WS_OG = WS_QG + 256 * 4096;
constexpr unsigned WS_PG = WS_OG + 256 * 1024;
constexpr unsigned WS_BYTES = WS_PG + 36 * 16 * 2 * 1024;

constexpr int L_WQ = 0;
constexpr int L_X = L_WQ + 18432;
constexpr int L_WO = L_X + 9216;
constexpr int L_RED = L_WO + 32768;
constexpr int L_OA = L_RED + NW * 1024;
constexpr int L_Q = L_OA + 16 * 528;
constexpr int L_ML = L_Q + 512;
constexpr int L_FLAG = L_ML + NW * 16;
constexpr int L_K0 = L_FLAG + 16;
constexpr int L_BYTES = L_K0 + NL * 1024;

constexpr unsigned SPIN_CAP = 1u << 18;
#define ST(i) do {} while (0)
#define ST_FLUSH() do {} while (0)

__device__ __forceinline__ float dot2(unsigned k, unsigned q, float acc) {
  return __builtin_amdgcn_fdot2_f32_bf16(__builtin_bit_cast(bf16x2, k), __builtin_bit_cast(bf16x2, q), acc, false);
}
__device__ __forceinline__ unsigned pkbf(float lo, float hi) {
  f32x2 f = {lo, hi};
  return __builtin_bit_cast(unsigned, __builtin_convertvector(f, bf16x2));
}
template <int CTRL> __device__ __forceinline__ float dpp(float x) {
  return __builtin_bit_cast(float, __builtin_amdgcn_mov_dpp(__builtin_bit_cast(int, x), CTRL, 0xf, 0xf, true));
}
constexpr int XOR1 = 0xB1, XOR2 = 0x4E, HMIRROR = 0x141, ROR8 = 0x128;
__device__ __forceinline__ float xrow_max(float x) {
  auto s = __builtin_amdgcn_permlane16_swap(__float_as_uint(x), __float_as_uint(x), false, false);
  x = fmaxf(__uint_as_float(s[0]), __uint_as_float(s[1]));
  auto t = __builtin_amdgcn_permlane32_swap(__float_as_uint(x), __float_as_uint(x), false, false);
  return fmaxf(__uint_as_float(t[0]), __uint_as_float(t[1]));
}
__device__ __forceinline__ float xrow_sum(float x) {
  auto s = __builtin_amdgcn_permlane16_swap(__float_as_uint(x), __float_as_uint(x), false, false);
  x = __uint_as_float(s[0]) + __uint_as_float(s[1]);
  auto t = __builtin_amdgcn_permlane32_swap(__float_as_uint(x), __float_as_uint(x), false, false);
  return __uint_as_float(t[0]) + __uint_as_float(t[1]);
}
__device__ __forceinline__ f32x4 mfma16(u32x4 a, u32x4 b, f32x4 c) {
  return __builtin_amdgcn_mfma_f32_16x16x32_bf16(__builtin_bit_cast(bf16x8, a), __builtin_bit_cast(bf16x8, b), c, 0, 0, 0);
}
#define W_NT 1
template <int NTP>
__device__ __forceinline__ void dma16(unsigned lds, unsigned voff, __amdgpu_buffer_rsrc_t rs, unsigned soff) {
  if constexpr (NTP)
    asm volatile("s_mov_b32 m0, %0\n\ts_nop 0\n\tbuffer_load_dwordx4 %1, %2, %3 offen nt lds"
                 :: "s"(lds), "v"(voff), "s"(rs), "s"(soff) : "memory");
  else
    asm volatile("s_mov_b32 m0, %0\n\ts_nop 0\n\tbuffer_load_dwordx4 %1, %2, %3 offen lds"
                 :: "s"(lds), "v"(voff), "s"(rs), "s"(soff) : "memory");
}
__device__ __forceinline__ u32x2 trread(LAS unsigned char* p) {
  s16x4 v = __builtin_amdgcn_ds_read_tr16_b64_v4i16((LAS s16x4*)p);
  return __builtin_bit_cast(u32x2, v);
}

__global__ void __launch_bounds__(NT, NW / 4)
fused_decode_kernel(const unsigned char* __restrict__ x, const unsigned char* __restrict__ kc,
                    const unsigned char* __restrict__ vc, const unsigned char* __restrict__ wq,
                    const unsigned char* __restrict__ wo, unsigned char* __restrict__ out,
                    unsigned char* ws) {
  __shared__ __attribute__((aligned(16))) unsigned char smem_[L_BYTES];
  LAS unsigned char* smem = (LAS unsigned char*)smem_;
  const unsigned lds0 = (unsigned)(size_t)smem;
  const int tid = threadIdx.x, lane = tid & 63;
  const int wave = __builtin_amdgcn_readfirstlane(tid >> 6);
  const int bid = blockIdx.x, jj = bid >> 3, kvh = 2 * (bid & 7) + (jj >> 4), b = jj & 15;
  const int li = lane & 15, lg = lane >> 4;
  ST(0);

  const __amdgpu_buffer_rsrc_t rs_ws = __builtin_amdgcn_make_buffer_rsrc(ws, 0, WS_BYTES, RSRC_W3);
  const size_t kvoff = ((size_t)(b * HKV + kvh) * SK + (SK - WIN)) * (DH * 2);
  const __amdgpu_buffer_rsrc_t rs_k = __builtin_amdgcn_make_buffer_rsrc((void*)(kc + kvoff), 0, WIN * DH * 2, RSRC_W3);
  const __amdgpu_buffer_rsrc_t rs_v = __builtin_amdgcn_make_buffer_rsrc((void*)(vc + kvoff), 0, WIN * DH * 2, RSRC_W3);

  volatile LAS unsigned* flag = (volatile LAS unsigned*)(smem + L_FLAG);
  const unsigned voff = (unsigned)tid * 16;
  u32x4 K[NL], V[NL];
#define LOADK(u) do { K[u] = __builtin_amdgcn_raw_buffer_load_b128(rs_k, voff, (u) * (NW * 1024), KV_AUX); __builtin_amdgcn_sched_barrier(0); } while (0)
#define LOADV(u) do { V[u] = __builtin_amdgcn_raw_buffer_load_b128(rs_v, voff, (u) * (NW * 1024), KV_AUX); __builtin_amdgcn_sched_barrier(0); } while (0)
#define PACE(n) do { __builtin_amdgcn_sched_barrier(0); asm volatile("s_waitcnt vmcnt(%0)" :: "i"((n) - 1) : "memory"); } while (0)
  if (wave == 0) {
    __builtin_amdgcn_s_setprio(3);
    const __amdgpu_buffer_rsrc_t rs_wq = __builtin_amdgcn_make_buffer_rsrc((void*)wq, 0, DM * NQ * 2, RSRC_W3);
    const __amdgpu_buffer_rsrc_t rs_x = __builtin_amdgcn_make_buffer_rsrc((void*)x, 0, NB * DM * 2, RSRC_W3);
    const unsigned vwq = (unsigned)((b >> 3) * 288 + (lane >> 2)) * (NQ * 2) + (unsigned)(lane & 3) * 16 + (unsigned)(kvh * 256 + 32 * (b & 7)) * 2;
    const unsigned vx = (unsigned)(lane >> 2) * (DM * 2) + (unsigned)(lane & 3) * 16 + (unsigned)(b >> 3) * 576;
#pragma unroll 1
    for (int s = 0; s < 9; ++s) {
      dma16<W_NT>(lds0 + L_WQ + s * 2048, vwq, rs_wq, (unsigned)s * (32 * NQ * 2));
      dma16<W_NT>(lds0 + L_WQ + s * 2048 + 1024, vwq, rs_wq, (unsigned)s * (32 * NQ * 2) + 16 * NQ * 2);
      dma16<X_NT>(lds0 + L_X + s * 1024, vx, rs_x, (unsigned)s * 64);
    }
    *flag = 0u;
  } else {
#pragma unroll
    for (int u = 0; u < KWIN; ++u) LOADK(u);
  }
  __builtin_amdgcn_sched_barrier(0);
  unsigned epoch = __builtin_amdgcn_raw_buffer_load_b32(rs_ws, WS_EPOCH, 0, 2);
  __builtin_amdgcn_sched_barrier(0);
  __syncthreads();
  ST(1);
  if (wave == 0) {
#pragma unroll
    for (int u = 0; u < KWIN; ++u) dma16<1>(lds0 + L_K0 + u * 1024, voff, rs_k, u * (NW * 1024));
    asm volatile("s_waitcnt vmcnt(%0)" :: "i"(KWIN) : "memory");
    ST(2);
    f32x4 a0 = {0.f, 0.f, 0.f, 0.f}, a1 = {0.f, 0.f, 0.f, 0.f};
    LAS unsigned char* pw = smem + L_WQ + (8 * lg + ((lane >> 2) & 3)) * 64 + (lane & 3) * 8;
    LAS unsigned char* px = smem + L_X + li * 64 + lg * 16;
#pragma unroll
    for (int s = 0; s < 9; ++s) {
      const u32x2 w0 = trread(pw + s * 2048), w1 = trread(pw + s * 2048 + 256);
      const u32x2 w2 = trread(pw + s * 2048 + 32), w3 = trread(pw + s * 2048 + 256 + 32);
      const u32x4 xb = *(LAS u32x4*)(px + s * 1024);
      a0 = mfma16(u32x4{w0[0], w0[1], w1[0], w1[1]}, xb, a0);
      a1 = mfma16(u32x4{w2[0], w2[1], w3[0], w3[1]}, xb, a1);
    }
    unsigned e0 = epoch;
    asm volatile("" : "+v"(e0));
    const unsigned tag = e0 + 1u;
    {
      const unsigned qo = WS_QG + (unsigned)(kvh * 16 + li) * 4096 + (unsigned)(b >> 3) * 2048 + (unsigned)(32 * (b & 7) + 4 * lg) * 8;
      __builtin_amdgcn_raw_buffer_store_b128(u32x4{__float_as_uint(a0[0]), tag, __float_as_uint(a0[1]), tag}, rs_ws, qo, 0, AUX_SC1);
      __builtin_amdgcn_raw_buffer_store_b128(u32x4{__float_as_uint(a0[2]), tag, __float_as_uint(a0[3]), tag}, rs_ws, qo + 16, 0, AUX_SC1);
      __builtin_amdgcn_raw_buffer_store_b128(u32x4{__float_as_uint(a1[0]), tag, __float_as_uint(a1[1]), tag}, rs_ws, qo + 128, 0, AUX_SC1);
      __builtin_amdgcn_raw_buffer_store_b128(u32x4{__float_as_uint(a1[2]), tag, __float_as_uint(a1[3]), tag}, rs_ws, qo + 144, 0, AUX_SC1);
    }
    ST(3);
    int u = KWIN;
    u32x4 r[4];
    const unsigned qi = WS_QG + (unsigned)(kvh * 16 + b) * 4096 + (unsigned)lane * 16;
    for (unsigned spins = 0;; ++spins) {
      if (u < NL) {
#pragma unroll
        for (int i = 0; i < KWIN; ++i) dma16<1>(lds0 + L_K0 + (u + i) * 1024, voff, rs_k, (u + i) * (NW * 1024));
        u += KWIN;
      }
      bool ok = true;
#pragma unroll
      for (int i = 0; i < 4; ++i) r[i] = __builtin_amdgcn_raw_buffer_load_b128(rs_ws, qi, i * 1024, AUX_SC1);
#pragma unroll
      for (int i = 0; i < 4; ++i) ok = ok & (r[i][1] == tag) & (r[i][3] == tag);
      if (__all(ok) || spins > SPIN_CAP) break;
      asm volatile("" ::: "memory");
    }
    *(LAS unsigned*)(smem + L_Q + lane * 4) = pkbf(__uint_as_float(r[0][0]) + __uint_as_float(r[2][0]), __uint_as_float(r[0][2]) + __uint_as_float(r[2][2]));
    *(LAS unsigned*)(smem + L_Q + 256 + lane * 4) = pkbf(__uint_as_float(r[1][0]) + __uint_as_float(r[3][0]), __uint_as_float(r[1][2]) + __uint_as_float(r[3][2]));
    asm volatile("s_waitcnt lgkmcnt(0)" ::: "memory");
    *flag = 1u;
    __builtin_amdgcn_s_setprio(0);
    ST(4);
#pragma unroll 1
    for (; u < NL; ++u) {
      dma16<1>(lds0 + L_K0 + u * 1024, voff, rs_k, u * (NW * 1024));
      PACE(KWIN);
    }
  } else {
    if (b < 9) {
      const __amdgpu_buffer_rsrc_t rs_wo = __builtin_amdgcn_make_buffer_rsrc((void*)wo, 0, NQ * DM * 2, RSRC_W3);
      const unsigned vwo = (unsigned)(lane >> 3) * (DM * 2) + (unsigned)(lane & 7) * 16;
#pragma unroll 1
      for (int i = wave - 1; i < 32; i += NW - 1) {
        PACE(KWIN);
        dma16<W_NT>(lds0 + L_WO + i * 1024, vwo, rs_wo, (unsigned)(kvh * 256 + 8 * i) * (DM * 2) + (unsigned)b * 128);
      }
    }
#pragma unroll
    for (int u = KWIN; u < NL; ++u) { PACE(KWIN); LOADK(u); }
  }
#pragma unroll
  for (int u = 0; u < NVPRE; ++u) { PACE(KWIN); LOADV(u); }
  if (wave == 0) {
    asm volatile("s_waitcnt vmcnt(%0)" :: "i"(NVPRE) : "memory");
#pragma unroll
    for (int u = 0; u < NL; ++u) K[u] = *(LAS u32x4*)(smem + L_K0 + u * 1024 + lane * 16);
  }
  ST(5);
  while (*flag == 0u) __builtin_amdgcn_s_sleep(1);
  asm volatile("" ::: "memory");
  ST(6);

  const int hA = li >> 3;
  const u32x4 qA = *(LAS u32x4*)(smem + L_Q + hA * 256 + li * 16);
  const u32x4 qB = *(LAS u32x4*)(smem + L_Q + (hA ^ 1) * 256 + li * 16);
  float y[NL];
  constexpr int NVB = NVPRE + NL / QPS + NVX;
#pragma unroll
  for (int u = 0; u < NL; ++u) {
    float xa = 0.f, xb = 0.f;
#pragma unroll
    for (int k = 0; k < 4; ++k) { xa = dot2(K[u][k], qA[k], xa); xb = dot2(K[u][k], qB[k], xb); }
    float t = xa + dpp<ROR8>(xb);
    t += dpp<XOR1>(t);
    t += dpp<XOR2>(t);
    t += dpp<HMIRROR>(t);
    y[u] = t;
    if (u % QPS == QPS - 1) { PACE(VWIN); LOADV(NVPRE + u / QPS); }
  }
  asm volatile("" : "+v"(epoch));
  const unsigned tag = epoch + 1u;
  constexpr float C2 = 0.088388347648318440f * 1.4426950408889634f;
  float m = y[0];
#pragma unroll
  for (int u = 1; u < NL; ++u) m = fmaxf(m, y[u]);
  m = xrow_max(m);
  const float mc = m * C2;
  float l = 0.f;
#pragma unroll
  for (int u = 0; u < NL; ++u) { y[u] = __builtin_amdgcn_exp2f(fmaf(y[u], C2, -mc)); l += y[u]; }
  l = xrow_sum(l);
  *(LAS f32x2*)(smem + L_ML + hA * (NW * 8) + wave * 8) = f32x2{mc, l};
  __builtin_amdgcn_sched_barrier(0);
#pragma unroll
  for (int u = NVB - NVX; u < NVB; ++u) LOADV(u);
  ST(7);
  __syncthreads();
  ST(8);
  {
    f32x4 ml[NW / 2];
#pragma unroll
    for (int w = 0; w < NW / 2; ++w) ml[w] = *(LAS f32x4*)(smem + L_ML + hA * (NW * 8) + w * 16);
    float M = fmaxf(ml[0][0], ml[0][2]);
#pragma unroll
    for (int w = 1; w < NW / 2; ++w) M = fmaxf(M, fmaxf(ml[w][0], ml[w][2]));
    float L = 0.f;
#pragma unroll
    for (int w = 0; w < NW / 2; ++w) {
      L = fmaf(ml[w][1], __builtin_amdgcn_exp2f(ml[w][0] - M), L);
      L = fmaf(ml[w][3], __builtin_amdgcn_exp2f(ml[w][2] - M), L);
    }
    const float f = __builtin_amdgcn_exp2f(mc - M) / L;
#pragma unroll
    for (int u = 0; u < NL; ++u) y[u] *= f;
  }
  const unsigned hm = (lane & 1) ? 0xffff0000u : 0x0000ffffu;
  const int rsel = (lane & 7) >> 1;
  const unsigned mk0 = rsel == 0 ? hm : 0u, mk1 = rsel == 1 ? hm : 0u, mk2 = rsel == 2 ? hm : 0u, mk3 = rsel == 3 ? hm : 0u;
  f32x4 acc = {0.f, 0.f, 0.f, 0.f};
#define PV(u) do { const unsigned pp = pkbf(y[u], y[u]); acc = mfma16(V[u], u32x4{pp & mk0, pp & mk1, pp & mk2, pp & mk3}, acc); } while (0)
#pragma unroll
  for (int u = 0; u < NL; ++u) {
    if (u + VWIN >= NVB && u + VWIN < NL) { PACE(VWIN); LOADV(u + VWIN); }
    PV(u);
  }
  *(LAS f32x4*)(smem + L_RED + wave * 1024 + lane * 16) = acc;
  ST(9);
  __syncthreads();
  ST(10);
  if (wave == 0) {
    f32x4 pr[NW];
#pragma unroll
    for (int w = 0; w < NW; ++w) pr[w] = *(LAS f32x4*)(smem + L_RED + w * 1024 + lane * 16);
    __builtin_amdgcn_sched_barrier(0);
#pragma unroll
    for (int st = 1; st < NW; st *= 2)
#pragma unroll
      for (int w = 0; w < NW; w += 2 * st) pr[w] += pr[w + st];
    const f32x4 o = pr[0];
    const u32x4 g = {pkbf(o[0], o[1]), tag, pkbf(o[2], o[3]), tag};
    __builtin_amdgcn_raw_buffer_store_b128(g, rs_ws, WS_OG + (unsigned)(kvh * 16 + b) * 1024 + (unsigned)lane * 16, 0, AUX_SC1);
  }
  ST(11);
  if (b < 9) {
    u32x4 wf[8];
    const int w4 = wave & 3;
    if (wave >= 4 && wave < 8) {
      LAS unsigned char* pw = smem + L_WO + (8 * ((lane >> 2) & 3) + 2 * lg) * 128 + w4 * 32 + (lane & 3) * 8;
#pragma unroll
      for (int s = 0; s < 8; ++s) {
        const int ro = ((s & 1) * 128 + 32 * (s >> 1)) * 128;
        const u32x2 w0 = trread(pw + ro), w1 = trread(pw + ro + 128);
        wf[s] = u32x4{w0[0], w0[1], w1[0], w1[1]};
      }
    }
#pragma unroll 1
    for (int pm = wave; pm < 16; pm += NW) {
      u32x4 r;
      for (unsigned spins = 0;; ++spins) {
        r = __builtin_amdgcn_raw_buffer_load_b128(rs_ws, WS_OG + (unsigned)(kvh * 16 + pm) * 1024 + (unsigned)lane * 16, 0, AUX_SC1);
        if (__all((r[1] == tag) & (r[3] == tag)) || spins > SPIN_CAP) break;
        __builtin_amdgcn_s_sleep(1);
        asm volatile("" ::: "memory");
      }
      *(LAS u32x2*)(smem + L_OA + pm * 528 + lane * 8) = u32x2{r[0], r[2]};
    }
    ST(12);
    __syncthreads();
    if (wave >= 4 && wave < 8) {
      f32x4 d = {0.f, 0.f, 0.f, 0.f};
#pragma unroll
      for (int s = 0; s < 8; ++s) {
        const u32x4 ob = *(LAS u32x4*)(smem + L_OA + li * 528 + s * 64 + lg * 16);
        d = mfma16(wf[s], ob, d);
      }
      const unsigned po = WS_PG + (unsigned)(((4 * b + w4) * 16 + kvh) * 2) * 1024 + (unsigned)lane * 16;
      __builtin_amdgcn_raw_buffer_store_b128(u32x4{__float_as_uint(d[0]), tag, __float_as_uint(d[1]), tag}, rs_ws, po, 0, AUX_SC1);
      __builtin_amdgcn_raw_buffer_store_b128(u32x4{__float_as_uint(d[2]), tag, __float_as_uint(d[3]), tag}, rs_ws, po + 1024, 0, AUX_SC1);
      ST(13);
    }
  } else if (wave == 1) {
    const int rho = (b - 9) * 16 + kvh;
    if (rho < 72) {
      const int T = rho >> 1, hf = rho & 1;
      const unsigned po = WS_PG + (unsigned)(T * 32 + hf) * 1024 + (unsigned)lane * 16;
      u32x4 r[16];
      for (unsigned spins = 0;; ++spins) {
        bool ok = true;
#pragma unroll
        for (int h = 0; h < 16; ++h) r[h] = __builtin_amdgcn_raw_buffer_load_b128(rs_ws, po, h * 2048, AUX_SC1);
#pragma unroll
        for (int h = 0; h < 16; ++h) ok = ok & (r[h][1] == tag) & (r[h][3] == tag);
        if (__all(ok) || spins > SPIN_CAP) break;
        __builtin_amdgcn_s_sleep(1);
        asm volatile("" ::: "memory");
      }
      ST(14);
      float s0 = 0.f, s1 = 0.f;
#pragma unroll
      for (int h = 0; h < 16; ++h) { s0 += __uint_as_float(r[h][0]); s1 += __uint_as_float(r[h][2]); }
      *(GAS unsigned*)(out + (size_t)(li * DM + 16 * T + 4 * lg + 2 * hf) * 2) = pkbf(s0, s1);
      if (rho == 0) __builtin_amdgcn_raw_buffer_store_b32(tag, rs_ws, WS_EPOCH, 0, AUX_SC1);
    }
  }
  ST(15);
  ST_FLUSH();
}

}

extern "C" void kernel_launch(void* const* d_in, const int* in_sizes, int n_in, void* d_out, int out_size,
                              void* d_ws, size_t ws_size, hipStream_t stream) {
  fused_decode_kernel<<<256, NT, 0, stream>>>((const unsigned char*)d_in[0], (const unsigned char*)d_in[1],
                                             (const unsigned char*)d_in[2], (const unsigned char*)d_in[3],
                                             (const unsigned char*)d_in[4], (unsigned char*)d_out,
                                             (unsigned char*)d_ws);
}
```

```cpp
#include <hip/hip_runtime.h>
#include <hip/hip_bf16.h>
#include <stdint.h>
#include <utility>

#define GAS __attribute__((address_space(1)))
#define LAS __attribute__((address_space(3)))
typedef unsigned u32x4 __attribute__((ext_vector_type(4)));
typedef unsigned u32x2 __attribute__((ext_vector_type(2)));
typedef float f32x4 __attribute__((ext_vector_type(4)));
typedef float f32x2 __attribute__((ext_vector_type(2)));
typedef short s16x8 __attribute__((ext_vector_type(8)));
typedef short s16x4 __attribute__((ext_vector_type(4)));
typedef __bf16 bf16x8 __attribute__((ext_vector_type(8)));
typedef __bf16 bf16x2 __attribute__((ext_vector_type(2)));

namespace {

constexpr int NB = 16, DM = 576, HKV = 16, SK = 4096, DH = 128, WIN = 1024, NQ = 4096;
#define NWAVES 8
constexpr int NW = NWAVES, NT = (NW + 1) * 64;
constexpr int NL = 256 / NW;
#define KWIN 3
#define VWIN 5
#define WWIN 3
#define T10 0
#define A10 280
#define TOFF 6
#define CODEPF 0
#define QS 26
#define FIN_AT 59
#define WOWIN 3
#define WO9A 10
#define QSLEEP 3
#define CHEAP 0
#define THR1 6
#define THR2 13
#define MAP 3
#define QSLEEP2 1
#define Q_ST 18
#define Q_LD 18
#define P_ST 16
#define P_LD 16
#define HO_ST 16
#define HO_LD 16
#define P1SLEEP 1
#define P2SLEEP 1
#define PRETOUCH 0
#define X_NT 0
#define KV_AUX 2
constexpr int AUX_SC1 = 16;
constexpr int RSRC_W3 = 0x00020000;

constexpr unsigned WS_EPOCH = 0;
constexpr unsigned WS_QG = 4096;
constexpr unsigned WS_OG = WS_QG + 256 * 1024;
constexpr unsigned WS_PG = WS_OG + 256 * 1024;
constexpr unsigned WS_BYTES = WS_PG + 36 * 16 * 2 * 1024;

constexpr int L_WQ = 0;
constexpr int L_X = L_WQ + 36864;
constexpr int L_WO = L_X + 18432;
constexpr int L_RED = L_WO + 32768;
constexpr int L_OA = L_RED + NW * 1024;
constexpr int L_Q = L_OA + 16 * 528;
constexpr int L_ML = L_Q + 512;
constexpr int L_FLAG = L_ML + NW * 16;
constexpr int L_BYTES = L_FLAG + 32;

constexpr unsigned SPIN_CAP = 1u << 18;
#define ST(i) do {} while (0)
#define ST_FLUSH() do {} while (0)

template <class F, int... I>
__device__ __forceinline__ void sfor_impl(F&& f, std::integer_sequence<int, I...>) { (f(std::integral_constant<int, I>{}), ...); }
template <int N, class F>
__device__ __forceinline__ void sfor(F&& f) { sfor_impl(f, std::make_integer_sequence<int, N>{}); }
constexpr int ES = QS + NL / 2, PS = ES + 4;
constexpr int npv_after(int slot) {
  int n = 0;
  for (int i = PS; i <= slot; ++i)
    for (int k = 0; k < 2; ++k)
      if (n <= i - NL - VWIN) ++n;
  return n;
}

__device__ __forceinline__ float dot2(unsigned k, unsigned q, float acc) {
  return __builtin_amdgcn_fdot2_f32_bf16(__builtin_bit_cast(bf16x2, k), __builtin_bit_cast(bf16x2, q), acc, false);
}
__device__ __forceinline__ unsigned pkbf(float lo, float hi) {
  f32x2 f = {lo, hi};
  return __builtin_bit_cast(unsigned, __builtin_convertvector(f, bf16x2));
}
template <int CTRL> __device__ __forceinline__ float dpp(float x) {
  return __builtin_bit_cast(float, __builtin_amdgcn_mov_dpp(__builtin_bit_cast(int, x), CTRL, 0xf, 0xf, true));
}
constexpr int XOR1 = 0xB1, XOR2 = 0x4E, HMIRROR = 0x141, ROR8 = 0x128;
__device__ __forceinline__ float xrow_max(float x) {
  auto s = __builtin_amdgcn_permlane16_swap(__float_as_uint(x), __float_as_uint(x), false, false);
  x = fmaxf(__uint_as_float(s[0]), __uint_as_float(s[1]));
  auto t = __builtin_amdgcn_permlane32_swap(__float_as_uint(x), __float_as_uint(x), false, false);
  return fmaxf(__uint_as_float(t[0]), __uint_as_float(t[1]));
}
__device__ __forceinline__ float xrow_sum(float x) {
  auto s = __builtin_amdgcn_permlane16_swap(__float_as_uint(x), __float_as_uint(x), false, false);
  x = __uint_as_float(s[0]) + __uint_as_float(s[1]);
  auto t = __builtin_amdgcn_permlane32_swap(__float_as_uint(x), __float_as_uint(x), false, false);
  return __uint_as_float(t[0]) + __uint_as_float(t[1]);
}
__device__ __forceinline__ f32x4 mfma16(u32x4 a, u32x4 b, f32x4 c) {
  return __builtin_amdgcn_mfma_f32_16x16x32_bf16(__builtin_bit_cast(bf16x8, a), __builtin_bit_cast(bf16x8, b), c, 0, 0, 0);
}
#define W_NT 1
template <int NTP>
__device__ __forceinline__ void dma16(unsigned lds, unsigned voff, __amdgpu_buffer_rsrc_t rs, unsigned soff) {
  if constexpr (NTP)
    asm volatile("s_mov_b32 m0, %0\n\ts_nop 0\n\tbuffer_load_dwordx4 %1, %2, %3 offen nt lds"
                 :: "s"(lds), "v"(voff), "s"(rs), "s"(soff) : "memory");
  else
    asm volatile("s_mov_b32 m0, %0\n\ts_nop 0\n\tbuffer_load_dwordx4 %1, %2, %3 offen lds"
                 :: "s"(lds), "v"(voff), "s"(rs), "s"(soff) : "memory");
}
__device__ __forceinline__ u32x2 trread(LAS unsigned char* p) {
  s16x4 v = __builtin_amdgcn_ds_read_tr16_b64_v4i16((LAS s16x4*)p);
  return __builtin_bit_cast(u32x2, v);
}

}

extern "C" __global__ void __launch_bounds__(NT, NW / 4 + 1)
fused_decode_kernel(const unsigned char* __restrict__ x, const unsigned char* __restrict__ kc,
                    const unsigned char* __restrict__ vc, const unsigned char* __restrict__ wq,
                    const unsigned char* __restrict__ wo, unsigned char* __restrict__ out,
                    unsigned char* ws) {
  __shared__ __attribute__((aligned(16))) unsigned char smem_[L_BYTES];
  LAS unsigned char* smem = (LAS unsigned char*)smem_;
  const unsigned lds0 = (unsigned)(size_t)smem;
  const int tid = threadIdx.x, lane = tid & 63;
  const int wave = __builtin_amdgcn_readfirstlane(tid >> 6);
  const int bid = blockIdx.x, jj = bid >> 3, b = jj & 15, kvh = 2 * ((bid + (b >> 1)) & 7) + (jj >> 4);
  const int li = lane & 15, lg = lane >> 4;
  ST(0);

  const __amdgpu_buffer_rsrc_t rs_ws = __builtin_amdgcn_make_buffer_rsrc(ws, 0, WS_BYTES, RSRC_W3);
  const size_t kvoff = ((size_t)(b * HKV + kvh) * SK + (SK - WIN)) * (DH * 2);
  const __amdgpu_buffer_rsrc_t rs_k = __builtin_amdgcn_make_buffer_rsrc((void*)(kc + kvoff), 0, WIN * DH * 2, RSRC_W3);
  const __amdgpu_buffer_rsrc_t rs_v = __builtin_amdgcn_make_buffer_rsrc((void*)(vc + kvoff), 0, WIN * DH * 2, RSRC_W3);

  volatile LAS unsigned* flag = (volatile LAS unsigned*)(smem + L_FLAG);
#define PACE(n) do { __builtin_amdgcn_sched_barrier(0); asm volatile("s_waitcnt vmcnt(%0)" :: "i"((n) - 1) : "memory"); } while (0)
  if (wave == 0) {
    flag[0] = 0u; flag[1] = 0u; flag[3] = 0u; flag[4] = 0u;
    unsigned cpf = 0;
    __builtin_amdgcn_s_setprio(3);
    __syncthreads();
    const __amdgpu_buffer_rsrc_t rs_wo = __builtin_amdgcn_make_buffer_rsrc((void*)wo, 0, NQ * DM * 2, RSRC_W3);
    const unsigned vwo = (unsigned)(lane >> 3) * (DM * 2) + (unsigned)(lane & 7) * 16;
#define WO9(i0, i1) _Pragma("unroll 1") for (int i = (i0); i < (i1); ++i) { PACE(WOWIN); \
        dma16<1>(lds0 + L_WQ + i * 1024, vwo, rs_wo, (unsigned)(kvh * 256 + 8 * i) * (DM * 2) + 8 * 128); }
    if (b >= 8) {
      const __amdgpu_buffer_rsrc_t rs_x = __builtin_amdgcn_make_buffer_rsrc((void*)x, 0, NB * DM * 2, RSRC_W3);
      const unsigned vx = (unsigned)(lane >> 2) * (DM * 2) + (unsigned)(lane & 3) * 16;
#pragma unroll 1
      for (int s = 0; s < 18; ++s) dma16<X_NT>(lds0 + L_X + s * 1024, vx, rs_x, (unsigned)s * 64);
    }
    unsigned epoch = __builtin_amdgcn_raw_buffer_load_b32(rs_ws, WS_EPOCH, 0, 2);
    ST(1);
    asm volatile("s_waitcnt vmcnt(0)" : "+v"(epoch) :: "memory");
    asm volatile("" :: "v"(cpf));
    const unsigned tag = epoch + 1u;
    flag[2] = tag;
    if (b >= 8) {
      while (flag[3] != (unsigned)NW) __builtin_amdgcn_s_sleep(1);
      asm volatile("" ::: "memory");
      ST(2);
      f32x4 a0 = {0.f, 0.f, 0.f, 0.f}, a1 = {0.f, 0.f, 0.f, 0.f};
      LAS unsigned char* pw = smem + L_WQ + (8 * lg + ((lane >> 2) & 3)) * 64 + (lane & 3) * 8;
      LAS unsigned char* px = smem + L_X + li * 64 + lg * 16;
#pragma unroll 6
      for (int s = 0; s < 18; ++s) {
        const u32x2 w0 = trread(pw + s * 2048), w1 = trread(pw + s * 2048 + 256);
        const u32x2 w2 = trread(pw + s * 2048 + 32), w3 = trread(pw + s * 2048 + 256 + 32);
        const u32x4 xa = *(LAS u32x4*)(px + s * 1024);
        a0 = mfma16(xa, u32x4{w0[0], w0[1], w1[0], w1[1]}, a0);
        a1 = mfma16(xa, u32x4{w2[0], w2[1], w3[0], w3[1]}, a1);
      }
      const unsigned qo = WS_QG + (unsigned)(kvh * 16 + 4 * lg) * 1024 + (unsigned)(b - 8) * 128 + (unsigned)li * 8;
#pragma unroll
      for (int r = 0; r < 4; ++r)
        __builtin_amdgcn_raw_buffer_store_b64(u32x2{pkbf(a0[r], a1[r]), tag}, rs_ws, qo, r * 1024, Q_ST);
      ST(3);
#pragma unroll 1
      for (int i = 0; i < QSLEEP2; ++i) __builtin_amdgcn_s_sleep(32);
    } else if (b == 0) {
      WO9(0, WO9A);
    } else {
#pragma unroll 1
      for (int i = 0; i < QSLEEP; ++i) __builtin_amdgcn_s_sleep(32);
    }
    const unsigned qi = WS_QG + (unsigned)(kvh * 16 + b) * 1024 + (unsigned)lane * 16;
    u32x4 qr;
    for (unsigned spins = 0;; ++spins) {
      asm volatile("" ::: "memory");
      qr = __builtin_amdgcn_raw_buffer_load_b128(rs_ws, qi, 0, Q_LD);
      if (__all((qr[1] == tag) & (qr[3] == tag)) || spins > SPIN_CAP) break;
      __builtin_amdgcn_s_sleep(4);
    }
    {
      LAS unsigned* ql = (LAS unsigned*)(smem + L_Q) + 16 * (lane >> 3) + (lane & 7);
      ql[0] = __builtin_amdgcn_perm(qr[2], qr[0], 0x05040100u);
      ql[8] = __builtin_amdgcn_perm(qr[2], qr[0], 0x07060302u);
    }
    asm volatile("s_waitcnt lgkmcnt(0)" ::: "memory");
    flag[0] = 1u;
    __builtin_amdgcn_s_setprio(0);
    ST(4);
    if (b == 0) WO9(WO9A, 32);
    asm volatile("s_waitcnt vmcnt(0)" ::: "memory");
    ST(9);
    __syncthreads();
    ST(10);
    f32x4 pr[NW];
#pragma unroll
    for (int w = 0; w < NW; ++w) pr[w] = *(LAS f32x4*)(smem + L_RED + w * 1024 + lane * 16);
    __builtin_amdgcn_sched_barrier(0);
#pragma unroll
    for (int st = 1; st < NW; st *= 2)
#pragma unroll
      for (int w = 0; w < NW; w += 2 * st) pr[w] += pr[w + st];
    const f32x4 o = pr[0];
    const u32x4 g = {pkbf(o[0], o[1]), tag, pkbf(o[2], o[3]), tag};
    __builtin_amdgcn_raw_buffer_store_b128(g, rs_ws, WS_OG + (unsigned)(kvh * 16 + b) * 1024 + (unsigned)lane * 16, 0, HO_ST);
    ST(11);
    ST(15);
    ST_FLUSH();
    return;
  }
  const int sw = wave - 1;
  const unsigned tb = (unsigned)__builtin_amdgcn_s_memrealtime();
  asm volatile("" :: "s"(out));
  {
    const bool isq = b >= 8;
    const __amdgpu_buffer_rsrc_t rs_w = __builtin_amdgcn_make_buffer_rsrc((void*)(isq ? wq : wo), 0, DM * NQ * 2, RSRC_W3);
    const unsigned vw = isq ? (unsigned)(lane >> 2) * (NQ * 2) + (unsigned)(lane & 3) * 16 : (unsigned)(lane >> 3) * (DM * 2) + (unsigned)(lane & 7) * 16;
    const unsigned s0 = isq ? (unsigned)(kvh * 256 + 32 * (b - 8)) * 2 : (unsigned)(kvh * 256) * (DM * 2) + (unsigned)b * 128;
    const unsigned sp = isq ? 16 * NQ * 2 : 8 * DM * 2;
    const unsigned l0 = lds0 + (isq ? L_WQ : L_WO);
    const int np = isq ? 36 : 32;
    int cnt = 0;
#pragma unroll 1
    for (int i = sw; i < np; i += NW) {
      if (cnt >= WWIN) PACE(WWIN);
      dma16<1>(l0 + i * 1024, vw, rs_w, s0 + (unsigned)i * sp);
      if (++cnt == 2) __syncthreads();
    }
  }
  ST(1);
  const unsigned voff = (unsigned)lane * 16 + (unsigned)sw * (NL * 1024);
  const unsigned ph = (unsigned)sw * 4096;
#define SOFF(u) (((u) * 1024 + ph) & (NL * 1024 - 1))
  u32x4 K[NL], V[NL], qA, qB;
  float y[NL], m = -INFINITY, mc = 0.f, l = 0.f, f = 0.f;
  unsigned tag = 0;
  const int hA = li >> 3;
  constexpr float C2 = 0.088388347648318440f * 1.4426950408889634f;
  const unsigned hm = (lane & 1) ? 0xffff0000u : 0x0000ffffu;
  const int rsel = (lane & 7) >> 1;
  const unsigned mk0 = rsel == 0 ? hm : 0u, mk1 = rsel == 1 ? hm : 0u, mk2 = rsel == 2 ? hm : 0u, mk3 = rsel == 3 ? hm : 0u;
  f32x4 acc = {0.f, 0.f, 0.f, 0.f};
#define PV(u) do { const unsigned pp = pkbf(y[u], y[u]); acc = mfma16(V[u], u32x4{pp & mk0, pp & mk1, pp & mk2, pp & mk3}, acc); } while (0)
  sfor<2 * NL>([&](auto ic) __attribute__((always_inline)) {
    constexpr int i = decltype(ic)::value;
    if constexpr (A10 > 0 && i >= KWIN) {
      PACE(VWIN);
      if ((unsigned)__builtin_amdgcn_s_memrealtime() - tb < (unsigned)((i + TOFF) * A10 / 10)) PACE(KWIN);
      __builtin_amdgcn_sched_barrier(0);
    } else if constexpr (i < QS) PACE(KWIN); else PACE(VWIN);
    if constexpr (T10 > 0) {
      while ((unsigned)__builtin_amdgcn_s_memrealtime() - tb < (unsigned)((i + TOFF) * T10 / 10)) __builtin_amdgcn_s_sleep(1);
      __builtin_amdgcn_sched_barrier(0);
    }
    if constexpr (i == KWIN - 1) {
      if (lane == 0) __hip_atomic_fetch_add((LAS unsigned*)(smem + L_FLAG + 12), 1u, __ATOMIC_RELAXED, __HIP_MEMORY_SCOPE_WORKGROUP);
    }
    if constexpr (i < NL) K[i] = __builtin_amdgcn_raw_buffer_load_b128(rs_k, voff, SOFF(i), KV_AUX);
    else V[i - NL] = __builtin_amdgcn_raw_buffer_load_b128(rs_v, voff, SOFF(i - NL), KV_AUX);
    __builtin_amdgcn_sched_barrier(0);
    if constexpr (i == QS) {
      ST(5);
      while (flag[0] == 0u) __builtin_amdgcn_s_sleep(1);
      asm volatile("" ::: "memory");
      tag = flag[2];
      qA = *(LAS u32x4*)(smem + L_Q + hA * 256 + li * 16);
      qB = *(LAS u32x4*)(smem + L_Q + (hA ^ 1) * 256 + li * 16);
      ST(6);
    }
    if constexpr (i >= QS && i < ES) {
#pragma unroll
      for (int u = 2 * (i - QS); u < 2 * (i - QS) + 2; ++u) {
        float xa = 0.f, xb = 0.f;
#pragma unroll
        for (int k = 0; k < 4; ++k) { xa = dot2(K[u][k], qA[k], xa); xb = dot2(K[u][k], qB[k], xb); }
        float t = xa + dpp<ROR8>(xb);
        t += dpp<XOR1>(t);
        t += dpp<XOR2>(t);
        t += dpp<HMIRROR>(t);
        y[u] = t;
        m = fmaxf(m, t);
      }
    }
    if constexpr (i == ES) mc = xrow_max(m) * C2;
    if constexpr (i >= ES && i < PS) {
#pragma unroll
      for (int u = (NL / 4) * (i - ES); u < (NL / 4) * (i - ES + 1); ++u) { y[u] = __builtin_amdgcn_exp2f(fmaf(y[u], C2, -mc)); l += y[u]; }
    }
    if constexpr (i == PS - 1) {
      l = xrow_sum(l);
      *(LAS f32x2*)(smem + L_ML + hA * (NW * 8) + sw * 8) = f32x2{mc, l};
      asm volatile("s_waitcnt lgkmcnt(0)" ::: "memory");
      if (lane == 0) __hip_atomic_fetch_add((LAS unsigned*)(smem + L_FLAG + 4), 1u, __ATOMIC_RELAXED, __HIP_MEMORY_SCOPE_WORKGROUP);
      ST(7);
    }
    if constexpr (i >= PS) {
#pragma unroll
      for (int u = npv_after(i - 1); u < npv_after(i); ++u) PV(u);
    }
    if constexpr (i == FIN_AT) {
      __builtin_amdgcn_sched_barrier(0);
      while (flag[1] != (unsigned)NW) __builtin_amdgcn_s_sleep(1);
      asm volatile("" ::: "memory");
      ST(8);
      f32x4 ml[NW / 2];
#pragma unroll
      for (int w = 0; w < NW / 2; ++w) ml[w] = *(LAS f32x4*)(smem + L_ML + hA * (NW * 8) + w * 16);
      float M = fmaxf(ml[0][0], ml[0][2]);
#pragma unroll
      for (int w = 1; w < NW / 2; ++w) M = fmaxf(M, fmaxf(ml[w][0], ml[w][2]));
      float L = 0.f;
#pragma unroll
      for (int w = 0; w < NW / 2; ++w) {
        L = fmaf(ml[w][1], __builtin_amdgcn_exp2f(ml[w][0] - M), L);
        L = fmaf(ml[w][3], __builtin_amdgcn_exp2f(ml[w][2] - M), L);
      }
      f = __builtin_amdgcn_exp2f(mc - M) / L;
    }
  });
  __builtin_amdgcn_sched_barrier(0);
#pragma unroll
  for (int u = npv_after(2 * NL - 1); u < NL; ++u) PV(u);
  *(LAS f32x4*)(smem + L_RED + sw * 1024 + lane * 16) = acc * f;
  ST(9);
  __syncthreads();
  ST(10);
  if (b < 8) {
    u32x4 wf[8];
    const int w4 = sw & 3;
    constexpr int NP = 16 / NW;
    u32x4 r[NP];
    const unsigned ob0 = WS_OG + (unsigned)(kvh * 16 + sw) * 1024;
    bool full = !CHEAP;
    unsigned oi = ob0 + (unsigned)(full ? lane : (lane & 7)) * 16;
#pragma unroll
    for (int i = 0; i < NP; ++i) r[i] = __builtin_amdgcn_raw_buffer_load_b128(rs_ws, oi, i * (NW * 1024), HO_LD);
    __builtin_amdgcn_sched_barrier(0);
    const bool tile = sw < 4 || b == 0;
    if (tile) {
      LAS unsigned char* pw = smem + (sw < 4 ? L_WO : L_WQ) + (8 * ((lane >> 2) & 3) + 2 * lg) * 128 + w4 * 32 + (lane & 3) * 8;
#pragma unroll
      for (int s = 0; s < 8; ++s) {
        const int ro = ((s & 1) * 128 + 32 * (s >> 1)) * 128;
        const u32x2 w0 = trread(pw + ro), w1 = trread(pw + ro + 128);
        wf[s] = u32x4{w0[0], w0[1], w1[0], w1[1]};
      }
    }
    __builtin_amdgcn_sched_barrier(0);
    for (unsigned spins = 0;; ++spins) {
      bool ok = true;
#pragma unroll
      for (int i = 0; i < NP; ++i) ok = ok & (r[i][1] == tag) & (r[i][3] == tag);
      const bool aok = __all(ok);
      if ((aok && full) || spins > SPIN_CAP) break;
      if (CHEAP && !full && (aok || __builtin_amdgcn_readfirstlane(flag[4]) >= THR1)) { full = true; oi = ob0 + (unsigned)lane * 16; }
      else if (P1SLEEP) __builtin_amdgcn_s_sleep(P1SLEEP);
      asm volatile("" ::: "memory");
#pragma unroll
      for (int i = 0; i < NP; ++i) r[i] = __builtin_amdgcn_raw_buffer_load_b128(rs_ws, oi, i * (NW * 1024), HO_LD);
    }
    if (CHEAP && lane == 0) __hip_atomic_fetch_add((LAS unsigned*)(smem + L_FLAG + 16), 1u, __ATOMIC_RELAXED, __HIP_MEMORY_SCOPE_WORKGROUP);
#pragma unroll
    for (int i = 0; i < NP; ++i) *(LAS u32x2*)(smem + L_OA + (sw + i * NW) * 528 + lane * 8) = u32x2{r[i][0], r[i][2]};
    ST(12);
    __syncthreads();
    if (tile) {
      f32x4 d = {0.f, 0.f, 0.f, 0.f};
#pragma unroll
      for (int s = 0; s < 8; ++s) {
        const u32x4 ob = *(LAS u32x4*)(smem + L_OA + li * 528 + s * 64 + lg * 16);
        d = mfma16(wf[s], ob, d);
      }
      const unsigned po = WS_PG + (unsigned)(((4 * (sw < 4 ? b : 8) + w4) * 16 + kvh) * 2) * 1024 + (unsigned)lane * 16;
      __builtin_amdgcn_raw_buffer_store_b128(u32x4{__float_as_uint(d[0]), tag, __float_as_uint(d[1]), tag}, rs_ws, po, 0, P_ST);
      __builtin_amdgcn_raw_buffer_store_b128(u32x4{__float_as_uint(d[2]), tag, __float_as_uint(d[3]), tag}, rs_ws, po + 1024, 0, P_ST);
      ST(13);
    }
  } else {
    const int rho = (b - 8) * 16 + kvh;
    if (rho < 72) {
      const int T = rho >> 1, hf = rho & 1;
      const int pl = 8 * sw + (lane & 7);
      const unsigned po = WS_PG + (unsigned)((T * 16 + (lane >> 3)) * 2 + hf) * 1024 + (unsigned)pl * 16;
      u32x4 r0, r1;
      if (CHEAP && sw != 0) {
        while (flag[4] < THR2) __builtin_amdgcn_s_sleep(1);
      }
      for (unsigned spins = 0;; ++spins) {
        r0 = __builtin_amdgcn_raw_buffer_load_b128(rs_ws, po, 0, P_LD);
        r1 = __builtin_amdgcn_raw_buffer_load_b128(rs_ws, po, 8 * 2048, P_LD);
        const unsigned long long b0 = __ballot((r0[1] == tag) & (r0[3] == tag)), b1 = __ballot((r1[1] == tag) & (r1[3] == tag));
        const bool done = (b0 & b1) == ~0ull || spins > SPIN_CAP;
        if (CHEAP && sw == 0) flag[4] = done ? 16u : (unsigned)(__builtin_popcountll(b0) + __builtin_popcountll(b1)) >> 3;
        if (done) break;
        if (P2SLEEP) __builtin_amdgcn_s_sleep(P2SLEEP);
        asm volatile("" ::: "memory");
      }
      ST(14);
      float s0 = __uint_as_float(r0[0]) + __uint_as_float(r1[0]), s1 = __uint_as_float(r0[2]) + __uint_as_float(r1[2]);
      s0 += dpp<ROR8>(s0); s1 += dpp<ROR8>(s1);
      s0 = xrow_sum(s0); s1 = xrow_sum(s1);
      if (lane < 8) *(GAS unsigned*)(out + (size_t)((pl & 15) * DM + 16 * T + 4 * (pl >> 4) + 2 * hf) * 2) = pkbf(s0, s1);
      if (rho == 0 && sw == 0) __builtin_amdgcn_raw_buffer_store_b32(tag, rs_ws, WS_EPOCH, 0, AUX_SC1);
    }
  }
  ST(15);
  ST_FLUSH();
}

extern "C" void kernel_launch(void* const* d_in, const int* in_sizes, int n_in, void* d_out, int out_size,
                              void* d_ws, size_t ws_size, hipStream_t stream) {
  const size_t ws_off = 0;
  fused_decode_kernel<<<256, NT, 0, stream>>>((const unsigned char*)d_in[0], (const unsigned char*)d_in[1],
                                             (const unsigned char*)d_in[2], (const unsigned char*)d_in[3],
                                             (const unsigned char*)d_in[4], (unsigned char*)d_out,
                                             (unsigned char*)d_ws + ws_off);
}
```

```cpp
#include <hip/hip_runtime.h>
#include <hip/hip_bf16.h>
#include <stdint.h>
#include <utility>

#define GAS __attribute__((address_space(1)))
#define LAS __attribute__((address_space(3)))
typedef unsigned u32x4 __attribute__((ext_vector_type(4)));
typedef unsigned u32x2 __attribute__((ext_vector_type(2)));
typedef float f32x4 __attribute__((ext_vector_type(4)));
typedef float f32x2 __attribute__((ext_vector_type(2)));
typedef short s16x4 __attribute__((ext_vector_type(4)));
typedef __bf16 bf16x8 __attribute__((ext_vector_type(8)));
typedef __bf16 bf16x2 __attribute__((ext_vector_type(2)));

namespace {

constexpr int NB = 16, DM = 576, HKV = 16, SK = 4096, DH = 128, WIN = 1024, NQ = 4096;
constexpr int NW = 8, NS = 4, NT = (NW + NS) * 64;
constexpr int NL = 256 / NW;

#define KWIN 3
#define VWIN 5
#define A10 280
#define TOFF 4
#define QS 26
#define FIN_AT 58
#define WOWIN 3
#define QSLEEP 3
#define QSLEEP2 1
#define GATE2 0
#define GATE2_MISS 0
#define GATE2_DLY 0
#define PIPE 0
#define PDELAY 6
constexpr int AUX_NT = 2, AUX_SC1 = 16;
constexpr int RSRC_W3 = 0x00020000;
constexpr unsigned OOB = 0x7ffffff0u;

constexpr unsigned WS_EPOCH = 0;
constexpr unsigned WS_QG = 4096;
constexpr unsigned WS_OG = WS_QG + 256 * 1024;
constexpr unsigned WS_PG = WS_OG + 256 * 1024;
constexpr unsigned WS_BYTES = WS_PG + 36 * 16 * 2 * 1024;

constexpr int L_WQ = 0;
constexpr int L_X = L_WQ + 36864;
constexpr int L_WO = L_X + 18432;
constexpr int L_RED = L_WO + 32768;
constexpr int L_OA = L_RED + NW * 1024;
constexpr int L_Q = L_OA + 16 * 528;
constexpr int L_ML = L_Q + 512;
constexpr int L_FLAG = L_ML + NW * 16;
constexpr int L_BYTES = L_FLAG + 32;

constexpr unsigned SPIN_CAP = 1u << 18;
#define ST(i) do {} while (0)
#define ST_FLUSH() do {} while (0)

template <class F, int... I>
__device__ __forceinline__ void sfor_impl(F&& f, std::integer_sequence<int, I...>) { (f(std::integral_constant<int, I>{}), ...); }
template <int N, class F>
__device__ __forceinline__ void sfor(F&& f) { sfor_impl(f, std::make_integer_sequence<int, N>{}); }
constexpr int ES = QS + NL / 2, PS = ES + 4;
constexpr int npv_after(int slot) {
  int n = 0;
  for (int i = PS; i <= slot; ++i)
    for (int k = 0; k < 2; ++k)
      if (n <= i - NL - VWIN) ++n;
  return n;
}

__device__ __forceinline__ float dot2(unsigned k, unsigned q, float acc) {
  return __builtin_amdgcn_fdot2_f32_bf16(__builtin_bit_cast(bf16x2, k), __builtin_bit_cast(bf16x2, q), acc, false);
}
__device__ __forceinline__ unsigned pkbf(float lo, float hi) {
  f32x2 f = {lo, hi};
  return __builtin_bit_cast(unsigned, __builtin_convertvector(f, bf16x2));
}
template <int CTRL> __device__ __forceinline__ float dpp(float x) {
  return __builtin_bit_cast(float, __builtin_amdgcn_mov_dpp(__builtin_bit_cast(int, x), CTRL, 0xf, 0xf, true));
}
constexpr int XOR1 = 0xB1, XOR2 = 0x4E, HMIRROR = 0x141, ROR8 = 0x128;
__device__ __forceinline__ float xrow_max(float x) {
  auto s = __builtin_amdgcn_permlane16_swap(__float_as_uint(x), __float_as_uint(x), false, false);
  x = fmaxf(__uint_as_float(s[0]), __uint_as_float(s[1]));
  auto t = __builtin_amdgcn_permlane32_swap(__float_as_uint(x), __float_as_uint(x), false, false);
  return fmaxf(__uint_as_float(t[0]), __uint_as_float(t[1]));
}
__device__ __forceinline__ float xrow_sum(float x) {
  auto s = __builtin_amdgcn_permlane16_swap(__float_as_uint(x), __float_as_uint(x), false, false);
  x = __uint_as_float(s[0]) + __uint_as_float(s[1]);
  auto t = __builtin_amdgcn_permlane32_swap(__float_as_uint(x), __float_as_uint(x), false, false);
  return __uint_as_float(t[0]) + __uint_as_float(t[1]);
}
__device__ __forceinline__ f32x4 mfma16(u32x4 a, u32x4 b, f32x4 c) {
  return __builtin_amdgcn_mfma_f32_16x16x32_bf16(__builtin_bit_cast(bf16x8, a), __builtin_bit_cast(bf16x8, b), c, 0, 0, 0);
}
template <int NTP>
__device__ __forceinline__ void dma16(unsigned lds, unsigned voff, __amdgpu_buffer_rsrc_t rs, unsigned soff) {
  if constexpr (NTP == 1)
    asm volatile("s_mov_b32 m0, %0\n\ts_nop 0\n\tbuffer_load_dwordx4 %1, %2, %3 offen nt lds"
                 :: "s"(lds), "v"(voff), "s"(rs), "s"(soff) : "memory");
  else
    asm volatile("s_mov_b32 m0, %0\n\ts_nop 0\n\tbuffer_load_dwordx4 %1, %2, %3 offen lds"
                 :: "s"(lds), "v"(voff), "s"(rs), "s"(soff) : "memory");
}
__device__ __forceinline__ u32x2 trread(LAS unsigned char* p) {
  s16x4 v = __builtin_amdgcn_ds_read_tr16_b64_v4i16((LAS s16x4*)p);
  return __builtin_bit_cast(u32x2, v);
}
template <int STRIDE>
__device__ __forceinline__ void poll4(unsigned (&p)[8], __amdgpu_buffer_rsrc_t rs, unsigned off, unsigned tag) {
  u32x4 r[4];
  for (unsigned spins = 0;; ++spins) {
    asm volatile("" ::: "memory");
#pragma unroll
    for (int i = 0; i < 4; ++i) r[i] = __builtin_amdgcn_raw_buffer_load_b128(rs, off, i * STRIDE, AUX_SC1);
    __builtin_amdgcn_sched_barrier(0);
    const unsigned d = (r[0][1] ^ tag) | (r[0][3] ^ tag) | (r[1][1] ^ tag) | (r[1][3] ^ tag) |
                       (r[2][1] ^ tag) | (r[2][3] ^ tag) | (r[3][1] ^ tag) | (r[3][3] ^ tag);
    if (__builtin_amdgcn_ballot_w64(d != 0u) == 0ull || spins >= SPIN_CAP) break;
  }
#pragma unroll
  for (int i = 0; i < 4; ++i) { p[2 * i] = r[i][0]; p[2 * i + 1] = r[i][2]; }
}

}

extern "C" __global__ void __launch_bounds__(NT, (NW + NS) / 4)
fused_decode_kernel(const unsigned char* __restrict__ x, const unsigned char* __restrict__ kc,
                    const unsigned char* __restrict__ vc, const unsigned char* __restrict__ wq,
                    const unsigned char* __restrict__ wo, unsigned char* out, unsigned char* ws) {
  __shared__ __attribute__((aligned(16))) unsigned char smem_[L_BYTES];
  LAS unsigned char* smem = (LAS unsigned char*)smem_;
  const unsigned lds0 = (unsigned)(size_t)smem;
  const int tid = threadIdx.x, lane = tid & 63;
  const int wave = __builtin_amdgcn_readfirstlane(tid >> 6);
  const int bid = blockIdx.x, jj = bid >> 3, b = jj & 15, kvh = 2 * ((bid + (b >> 1)) & 7) + (jj >> 4);
  const int li = lane & 15, lg = lane >> 4;
  ST(0);
  volatile LAS unsigned* flag = (volatile LAS unsigned*)(smem + L_FLAG);
#define PACE(n) do { __builtin_amdgcn_sched_barrier(0); asm volatile("s_waitcnt vmcnt(%0)" :: "i"((n) - 1) : "memory"); } while (0)

  if (wave >= NW) {
    const int t = wave - NW;
    const __amdgpu_buffer_rsrc_t rs_ws = __builtin_amdgcn_make_buffer_rsrc(ws, 0, WS_BYTES, RSRC_W3);
    if (t == 0) { flag[0] = 0u; flag[1] = 0u; flag[3] = 0u; flag[4] = 0u; flag[5] = 0u; }
    __builtin_amdgcn_s_setprio(3);
    __syncthreads();
    if (t == 0) {
      if (b >= 8) {
        const __amdgpu_buffer_rsrc_t rs_x = __builtin_amdgcn_make_buffer_rsrc((void*)x, 0, NB * DM * 2, RSRC_W3);
        const unsigned vx = (unsigned)(lane >> 2) * (DM * 2) + (unsigned)(lane & 3) * 16;
#pragma unroll 1
        for (int s = 0; s < 18; ++s) dma16<0>(lds0 + L_X + s * 1024, vx, rs_x, (unsigned)s * 64);
      }
      unsigned epoch = __builtin_amdgcn_raw_buffer_load_b32(rs_ws, WS_EPOCH, 0, AUX_NT);
      ST(1);
      asm volatile("s_waitcnt vmcnt(0)" : "+v"(epoch) :: "memory");
      const unsigned tg = epoch + 1u;
      flag[2] = tg;
      if (b >= 8) {
        while (flag[3] != (unsigned)NW) __builtin_amdgcn_s_sleep(1);
        asm volatile("" ::: "memory");
        ST(2);
        f32x4 a0 = {0.f, 0.f, 0.f, 0.f}, a1 = {0.f, 0.f, 0.f, 0.f};
        LAS unsigned char* pw = smem + L_WQ + (8 * lg + ((lane >> 2) & 3)) * 64 + (lane & 3) * 8;
        LAS unsigned char* px = smem + L_X + li * 64 + lg * 16;
#pragma unroll 6
        for (int s = 0; s < 18; ++s) {
          const u32x2 w0 = trread(pw + s * 2048), w1 = trread(pw + s * 2048 + 256);
          const u32x2 w2 = trread(pw + s * 2048 + 32), w3 = trread(pw + s * 2048 + 256 + 32);
          const u32x4 xa = *(LAS u32x4*)(px + s * 1024);
          a0 = mfma16(xa, u32x4{w0[0], w0[1], w1[0], w1[1]}, a0);
          a1 = mfma16(xa, u32x4{w2[0], w2[1], w3[0], w3[1]}, a1);
        }
        const unsigned qo = WS_QG + (unsigned)(kvh * 16 + 4 * lg) * 1024 + (unsigned)(b - 8) * 128 + (unsigned)li * 8;
#pragma unroll
        for (int r = 0; r < 4; ++r)
          __builtin_amdgcn_raw_buffer_store_b64(u32x2{pkbf(a0[r], a1[r]), tg}, rs_ws, qo, r * 1024, AUX_SC1 | AUX_NT);
        ST(3);
#pragma unroll 1
        for (int i = 0; i < QSLEEP2; ++i) __builtin_amdgcn_s_sleep(32);
      } else {
#pragma unroll 1
        for (int i = 0; i < QSLEEP; ++i) __builtin_amdgcn_s_sleep(32);
      }
      const unsigned qi = WS_QG + (unsigned)(kvh * 16 + b) * 1024 + (unsigned)lane * 16;
      u32x4 qr;
      for (unsigned spins = 0;; ++spins) {
        asm volatile("" ::: "memory");
        qr = __builtin_amdgcn_raw_buffer_load_b128(rs_ws, qi, 0, AUX_SC1 | AUX_NT);
        if (__builtin_amdgcn_ballot_w64(((qr[1] ^ tg) | (qr[3] ^ tg)) != 0u) == 0ull || spins > SPIN_CAP) break;
        __builtin_amdgcn_s_sleep(4);
      }
      {
        LAS unsigned* ql = (LAS unsigned*)(smem + L_Q) + 16 * (lane >> 3) + (lane & 7);
        ql[0] = __builtin_amdgcn_perm(qr[2], qr[0], 0x05040100u);
        ql[8] = __builtin_amdgcn_perm(qr[2], qr[0], 0x07060302u);
      }
      asm volatile("s_waitcnt lgkmcnt(0)" ::: "memory");
      flag[0] = 1u;
      ST(4);
    } else if (t == 1 && b == 0) {
      const __amdgpu_buffer_rsrc_t rs_wo = __builtin_amdgcn_make_buffer_rsrc((void*)wo, 0, NQ * DM * 2, RSRC_W3);
      const unsigned vwo = (unsigned)(lane >> 3) * (DM * 2) + (unsigned)(lane & 7) * 16;
#pragma unroll 1
      for (int i = 0; i < 32; ++i) {
        PACE(WOWIN);
        dma16<1>(lds0 + L_WQ + i * 1024, vwo, rs_wo, (unsigned)(kvh * 256 + 8 * i) * (DM * 2) + 8 * 128);
      }
      asm volatile("s_waitcnt vmcnt(0)" ::: "memory");
      flag[4] = 1u;
    }
    while (flag[0] == 0u) __builtin_amdgcn_s_sleep(16);
    asm volatile("" ::: "memory");
    const unsigned tag = flag[2];
    const __amdgpu_buffer_rsrc_t rs_out = __builtin_amdgcn_make_buffer_rsrc(out, 0, NB * DM * 2, RSRC_W3);
    u32x4 wf[8], wf9[8];
    LAS unsigned char* pwf = smem + (8 * ((lane >> 2) & 3) + 2 * lg) * 128 + t * 32 + (lane & 3) * 8;
#define LOADWF(dst, base) _Pragma("unroll") for (int s = 0; s < 8; ++s) { const int ro = ((s & 1) * 128 + 32 * (s >> 1)) * 128; \
        const u32x2 w0 = trread(pwf + (base) + ro), w1 = trread(pwf + (base) + ro + 128); dst[s] = u32x4{w0[0], w0[1], w1[0], w1[1]}; }
    if (b == 0) {
      while (flag[4] == 0u) __builtin_amdgcn_s_sleep(16);
      asm volatile("" ::: "memory");
      LOADWF(wf9, L_WQ)
    }
    const int rho = (b - 8) * 16 + kvh, T = rho >> 1, hf = rho & 1;
    unsigned o_pub = WS_OG + (unsigned)(kvh * 16 + b) * 1024 + (unsigned)lane * 16;
    unsigned o_get = b < 8 ? WS_OG + (unsigned)(kvh * 16 + 4 * t) * 1024 + (unsigned)lane * 16
                           : WS_PG + (unsigned)((T * 16 + lg) * 2 + hf) * 1024 + (unsigned)(16 * t + li) * 16;
    unsigned o_put = b < 8 ? WS_PG + (unsigned)(((4 * b + t) * 16 + kvh) * 2) * 1024 + (unsigned)lane * 16
                           : (lg == 0 ? (unsigned)(li * DM + 16 * T + 4 * t + 2 * hf) * 2 : OOB);
    asm volatile("" : "+v"(o_pub), "+v"(o_get), "+v"(o_put));
    if (b < 8 && t > 0) {
      const __amdgpu_buffer_rsrc_t rs_wo = __builtin_amdgcn_make_buffer_rsrc((void*)wo, 0, NQ * DM * 2, RSRC_W3);
      const unsigned vwo = (unsigned)(lane >> 3) * (DM * 2) + (unsigned)(lane & 7) * 16;
      while (flag[5] != (unsigned)NW) __builtin_amdgcn_s_sleep(2);
#pragma unroll 1
      for (int i = t - 1; i < 32; i += 3)
        dma16<1>(lds0 + L_WO + i * 1024, vwo, rs_wo, (unsigned)(kvh * 256 + 8 * i) * (DM * 2) + (unsigned)b * 128);
    }
    ST(9);
    __syncthreads();
    ST(10);
    if (t == 0) {
      f32x4 pr[NW];
#pragma unroll
      for (int w = 0; w < NW; ++w) pr[w] = *(LAS f32x4*)(smem + L_RED + w * 1024 + lane * 16);
      __builtin_amdgcn_sched_barrier(0);
#pragma unroll
      for (int s = 1; s < NW; s *= 2)
#pragma unroll
        for (int w = 0; w < NW; w += 2 * s) pr[w] += pr[w + s];
      const f32x4 o = pr[0];
      __builtin_amdgcn_raw_buffer_store_b128(u32x4{pkbf(o[0], o[1]), tag, pkbf(o[2], o[3]), tag}, rs_ws, o_pub, 0, AUX_SC1);
      ST(11);
    }
    unsigned p[8];
    if (b < 8) {
      if (t > 0) {
        asm volatile("s_waitcnt vmcnt(0)" ::: "memory");
        if (lane == 0) __hip_atomic_fetch_add((LAS unsigned*)(smem + L_FLAG + 12), 1u, __ATOMIC_RELAXED, __HIP_MEMORY_SCOPE_WORKGROUP);
      }
      while (flag[3] != 3u) __builtin_amdgcn_s_sleep(1);
      asm volatile("" ::: "memory");
      ST(8);
      LOADWF(wf, L_WO)
      __builtin_amdgcn_sched_barrier(0);
      poll4<1024>(p, rs_ws, o_get, tag);
#pragma unroll
      for (int i = 0; i < 4; ++i) *(LAS u32x2*)(smem + L_OA + (4 * t + i) * 528 + lane * 8) = u32x2{p[2 * i], p[2 * i + 1]};
      ST(12);
      __syncthreads();
      u32x4 ob[8];
#pragma unroll
      for (int s = 0; s < 8; ++s) ob[s] = *(LAS u32x4*)(smem + L_OA + li * 528 + s * 64 + lg * 16);
      __builtin_amdgcn_sched_barrier(0);
      f32x4 d = {0.f, 0.f, 0.f, 0.f};
#pragma unroll
      for (int s = 0; s < 8; ++s) d = mfma16(wf[s], ob[s], d);
      __builtin_amdgcn_raw_buffer_store_b128(u32x4{__float_as_uint(d[0]), tag, __float_as_uint(d[1]), tag}, rs_ws, o_put, 0, AUX_SC1);
      __builtin_amdgcn_raw_buffer_store_b128(u32x4{__float_as_uint(d[2]), tag, __float_as_uint(d[3]), tag}, rs_ws, o_put, 1024, AUX_SC1);
      if (b == 0) {
        f32x4 e = {0.f, 0.f, 0.f, 0.f};
#pragma unroll
        for (int s = 0; s < 8; ++s) e = mfma16(wf9[s], ob[s], e);
        __builtin_amdgcn_raw_buffer_store_b128(u32x4{__float_as_uint(e[0]), tag, __float_as_uint(e[1]), tag}, rs_ws, o_put, 32 * 32768, AUX_SC1);
        __builtin_amdgcn_raw_buffer_store_b128(u32x4{__float_as_uint(e[2]), tag, __float_as_uint(e[3]), tag}, rs_ws, o_put, 32 * 32768 + 1024, AUX_SC1);
      }
      ST(13);
    } else if (rho < 72) {
      poll4<4 * 2048>(p, rs_ws, o_get, tag);
      ST(14);
      float s0 = (__uint_as_float(p[0]) + __uint_as_float(p[2])) + (__uint_as_float(p[4]) + __uint_as_float(p[6]));
      float s1 = (__uint_as_float(p[1]) + __uint_as_float(p[3])) + (__uint_as_float(p[5]) + __uint_as_float(p[7]));
      s0 = xrow_sum(s0); s1 = xrow_sum(s1);
      __builtin_amdgcn_raw_buffer_store_b32(pkbf(s0, s1), rs_out, o_put, 0, 0);
      if (rho == 0 && t == 0) __builtin_amdgcn_raw_buffer_store_b32(tag, rs_ws, WS_EPOCH, 0, AUX_SC1);
      ST(15);
    }
    ST_FLUSH();
    return;
  }

  const int sw = wave;
  const unsigned tb = (unsigned)__builtin_amdgcn_s_memrealtime();
  const bool isq = b >= 8;
  if (isq) {
    const __amdgpu_buffer_rsrc_t rs_w = __builtin_amdgcn_make_buffer_rsrc((void*)wq, 0, DM * NQ * 2, RSRC_W3);
    const unsigned vw = (unsigned)(lane >> 2) * (NQ * 2) + (unsigned)(lane & 3) * 16 + (unsigned)(kvh * 256 + 32 * (b - 8)) * 2;
    int cnt = 0;
#pragma unroll 1
    for (int i = sw; i < 36; i += NW) {
      if (cnt >= KWIN) PACE(KWIN);
      dma16<1>(lds0 + L_WQ + i * 1024, vw, rs_w, (unsigned)i * (16 * NQ * 2));
      if (++cnt == KWIN) __syncthreads();
    }
    static_assert(KWIN <= 4, "every streaming wave must reach the barrier inside the weight loop");
  }
  ST(1);
  const size_t kvoff = ((size_t)(b * HKV + kvh) * SK + (SK - WIN)) * (DH * 2);
  const __amdgpu_buffer_rsrc_t rs_k = __builtin_amdgcn_make_buffer_rsrc((void*)(kc + kvoff), 0, WIN * DH * 2, RSRC_W3);
  const __amdgpu_buffer_rsrc_t rs_v = __builtin_amdgcn_make_buffer_rsrc((void*)(vc + kvoff), 0, WIN * DH * 2, RSRC_W3);
  const unsigned voff = (unsigned)lane * 16 + (unsigned)sw * (NL * 1024);
  const unsigned ph = (unsigned)sw * 4096;
#define SOFF(u) (((u) * 1024 + ph) & (NL * 1024 - 1))
  u32x4 K[NL], V[NL], qA, qB;
  float y[NL], m = -INFINITY, mc = 0.f, l = 0.f, f = 0.f;
  const int hA = li >> 3;
  constexpr float C2 = 0.088388347648318440f * 1.4426950408889634f;
  const unsigned hm = (lane & 1) ? 0xffff0000u : 0x0000ffffu;
  const int rsel = (lane & 7) >> 1;
  const unsigned mk0 = rsel == 0 ? hm : 0u, mk1 = rsel == 1 ? hm : 0u, mk2 = rsel == 2 ? hm : 0u, mk3 = rsel == 3 ? hm : 0u;
  f32x4 acc = {0.f, 0.f, 0.f, 0.f};
#define PV(u) do { const unsigned pp = pkbf(y[u], y[u]); acc = mfma16(V[u], u32x4{pp & mk0, pp & mk1, pp & mk2, pp & mk3}, acc); } while (0)
  sfor<2 * NL>([&](auto ic) __attribute__((always_inline)) {
    constexpr int i = decltype(ic)::value;
    if constexpr (i < KWIN) PACE(KWIN);
    else {
      PACE(VWIN);
      if ((unsigned)__builtin_amdgcn_s_memrealtime() - tb < (unsigned)((i + TOFF) * A10 / 10)) PACE(KWIN);
      __builtin_amdgcn_sched_barrier(0);
    }
    if constexpr (i == KWIN - 1) {
      if (!isq) __syncthreads();
      else if (lane == 0) __hip_atomic_fetch_add((LAS unsigned*)(smem + L_FLAG + 12), 1u, __ATOMIC_RELAXED, __HIP_MEMORY_SCOPE_WORKGROUP);
    }
    if constexpr (i < NL) K[i] = __builtin_amdgcn_raw_buffer_load_b128(rs_k, voff, SOFF(i), AUX_NT);
    else V[i - NL] = __builtin_amdgcn_raw_buffer_load_b128(rs_v, voff, SOFF(i - NL), AUX_NT);
    __builtin_amdgcn_sched_barrier(0);
    if constexpr (i == 2 * NL - 1) {
      if (lane == 0) __hip_atomic_fetch_add((LAS unsigned*)(smem + L_FLAG + 20), 1u, __ATOMIC_RELAXED, __HIP_MEMORY_SCOPE_WORKGROUP);
      __builtin_amdgcn_sched_barrier(0);
    }
    if constexpr (i == QS) {
      ST(5);
      while (flag[0] == 0u) __builtin_amdgcn_s_sleep(1);
      asm volatile("" ::: "memory");
      qA = *(LAS u32x4*)(smem + L_Q + hA * 256 + li * 16);
      qB = *(LAS u32x4*)(smem + L_Q + (hA ^ 1) * 256 + li * 16);
      ST(6);
    }
    if constexpr (i >= QS && i < ES) {
#pragma unroll
      for (int u = 2 * (i - QS); u < 2 * (i - QS) + 2; ++u) {
        float xa = 0.f, xb = 0.f;
#pragma unroll
        for (int k = 0; k < 4; ++k) { xa = dot2(K[u][k], qA[k], xa); xb = dot2(K[u][k], qB[k], xb); }
        float t = xa + dpp<ROR8>(xb);
        t += dpp<XOR1>(t);
        t += dpp<XOR2>(t);
        t += dpp<HMIRROR>(t);
        y[u] = t;
        m = fmaxf(m, t);
        asm volatile("" : "+v"(y[u]), "+v"(m));
      }
    }
    if constexpr (i == ES) mc = xrow_max(m) * C2;
    if constexpr (i >= ES && i < PS) {
#pragma unroll
      for (int u = (NL / 4) * (i - ES); u < (NL / 4) * (i - ES + 1); ++u) { y[u] = __builtin_amdgcn_exp2f(fmaf(y[u], C2, -mc)); l += y[u]; }
      asm volatile("" : "+v"(l));
    }
    if constexpr (i == PS - 1) {
      l = xrow_sum(l);
      *(LAS f32x2*)(smem + L_ML + hA * (NW * 8) + sw * 8) = f32x2{mc, l};
      asm volatile("s_waitcnt lgkmcnt(0)" ::: "memory");
      if (lane == 0) __hip_atomic_fetch_add((LAS unsigned*)(smem + L_FLAG + 4), 1u, __ATOMIC_RELAXED, __HIP_MEMORY_SCOPE_WORKGROUP);
      ST(7);
    }
    if constexpr (i >= PS) {
#pragma unroll
      for (int u = npv_after(i - 1); u < npv_after(i); ++u) PV(u);
      asm volatile("" : "+v"(acc));
    }
    if constexpr (i == FIN_AT) {
      __builtin_amdgcn_sched_barrier(0);
      while (flag[1] != (unsigned)NW) __builtin_amdgcn_s_sleep(1);
      asm volatile("" ::: "memory");
      ST(8);
      f32x4 ml[NW / 2];
#pragma unroll
      for (int w = 0; w < NW / 2; ++w) ml[w] = *(LAS f32x4*)(smem + L_ML + hA * (NW * 8) + w * 16);
      float M = fmaxf(ml[0][0], ml[0][2]);
#pragma unroll
      for (int w = 1; w < NW / 2; ++w) M = fmaxf(M, fmaxf(ml[w][0], ml[w][2]));
      float L = 0.f;
#pragma unroll
      for (int w = 0; w < NW / 2; ++w) {
        L = fmaf(ml[w][1], __builtin_amdgcn_exp2f(ml[w][0] - M), L);
        L = fmaf(ml[w][3], __builtin_amdgcn_exp2f(ml[w][2] - M), L);
      }
      f = __builtin_amdgcn_exp2f(mc - M) / L;
      asm volatile("" : "+v"(f));
    }
  });
  __builtin_amdgcn_sched_barrier(0);
#pragma unroll
  for (int u = npv_after(2 * NL - 1); u < NL; ++u) PV(u);
  *(LAS f32x4*)(smem + L_RED + sw * 1024 + lane * 16) = acc * f;
  ST(9);
  __syncthreads();
  ST(10);
  ST_FLUSH();
}

extern "C" void kernel_launch(void* const* d_in, const int* in_sizes, int n_in, void* d_out, int out_size,
                              void* d_ws, size_t ws_size, hipStream_t stream) {
  fused_decode_kernel<<<256, NT, 0, stream>>>((const unsigned char*)d_in[0], (const unsigned char*)d_in[1],
                                             (const unsigned char*)d_in[2], (const unsigned char*)d_in[3],
                                             (const unsigned char*)d_in[4], (unsigned char*)d_out,
                                             (unsigned char*)d_ws);
}
```

```cpp
#include <hip/hip_runtime.h>
#include <hip/hip_bf16.h>
#include <stdint.h>
#include <utility>

#define GAS __attribute__((address_space(1)))
#define LAS __attribute__((address_space(3)))
typedef unsigned u32x4 __attribute__((ext_vector_type(4)));
typedef unsigned u32x2 __attribute__((ext_vector_type(2)));
typedef float f32x4 __attribute__((ext_vector_type(4)));
typedef float f32x2 __attribute__((ext_vector_type(2)));
typedef short s16x4 __attribute__((ext_vector_type(4)));
typedef __bf16 bf16x8 __attribute__((ext_vector_type(8)));
typedef __bf16 bf16x2 __attribute__((ext_vector_type(2)));

namespace {

constexpr int NB = 16, DM = 576, HKV = 16, SK = 4096, DH = 128, WIN = 1024, NQ = 4096;
constexpr int NW = 8, NS = 4, NT = (NW + NS) * 64;
constexpr int NL = 256 / NW;

constexpr int KWIN = 3, VWIN = 5;
constexpr int SLOT_NS = 283, TOFF = 4;
constexpr int QS = 26;
constexpr int FIN_AT = 58;
constexpr int WOWIN = 3;
constexpr int AUX_NT = 2, AUX_SC1 = 16;
constexpr int RSRC_W3 = 0x00020000;
constexpr unsigned OOB = 0x7ffffff0u;

constexpr unsigned WS_EPOCH = 0;
constexpr unsigned WS_QG = 4096;
constexpr unsigned WS_OG = WS_QG + 256 * 1024;
constexpr unsigned WS_PG = WS_OG + 256 * 1024;
constexpr unsigned WS_BYTES = WS_PG + 36 * 16 * 2 * 1024;

constexpr int L_WQ = 0;
constexpr int L_X = L_WQ + 36864;
constexpr int L_WO = L_X + 18432;
constexpr int L_RED = L_WO + 32768;
constexpr int L_OA = L_RED + NW * 1024;
constexpr int L_Q = L_OA + 16 * 528;
constexpr int L_ML = L_Q + 512;
constexpr int L_FLAG = L_ML + NW * 16;
constexpr int L_BYTES = L_FLAG + 32;

constexpr unsigned SPIN_CAP = 1u << 18;

template <class F, int... I>
__device__ __forceinline__ void sfor_impl(F&& f, std::integer_sequence<int, I...>) { (f(std::integral_constant<int, I>{}), ...); }
template <int N, class F>
__device__ __forceinline__ void sfor(F&& f) { sfor_impl(f, std::make_integer_sequence<int, N>{}); }
constexpr int ES = QS + NL / 2, PS = ES + 4;
constexpr int npv_after(int slot) {
  int n = 0;
  for (int i = PS; i <= slot; ++i)
    for (int k = 0; k < 2; ++k)
      if (n <= i - NL - VWIN) ++n;
  return n;
}

__device__ __forceinline__ float dot2(unsigned k, unsigned q, float acc) {
  return __builtin_amdgcn_fdot2_f32_bf16(__builtin_bit_cast(bf16x2, k), __builtin_bit_cast(bf16x2, q), acc, false);
}
__device__ __forceinline__ unsigned pkbf(float lo, float hi) {
  f32x2 f = {lo, hi};
  return __builtin_bit_cast(unsigned, __builtin_convertvector(f, bf16x2));
}
template <int CTRL> __device__ __forceinline__ float dpp(float x) {
  return __builtin_bit_cast(float, __builtin_amdgcn_mov_dpp(__builtin_bit_cast(int, x), CTRL, 0xf, 0xf, true));
}
constexpr int XOR1 = 0xB1, XOR2 = 0x4E, HMIRROR = 0x141, ROR8 = 0x128;
__device__ __forceinline__ float xrow_max(float x) {
  auto s = __builtin_amdgcn_permlane16_swap(__float_as_uint(x), __float_as_uint(x), false, false);
  x = fmaxf(__uint_as_float(s[0]), __uint_as_float(s[1]));
  auto t = __builtin_amdgcn_permlane32_swap(__float_as_uint(x), __float_as_uint(x), false, false);
  return fmaxf(__uint_as_float(t[0]), __uint_as_float(t[1]));
}
__device__ __forceinline__ float xrow_sum(float x) {
  auto s = __builtin_amdgcn_permlane16_swap(__float_as_uint(x), __float_as_uint(x), false, false);
  x = __uint_as_float(s[0]) + __uint_as_float(s[1]);
  auto t = __builtin_amdgcn_permlane32_swap(__float_as_uint(x), __float_as_uint(x), false, false);
  return __uint_as_float(t[0]) + __uint_as_float(t[1]);
}
__device__ __forceinline__ f32x4 mfma16(u32x4 a, u32x4 b, f32x4 c) {
  return __builtin_amdgcn_mfma_f32_16x16x32_bf16(__builtin_bit_cast(bf16x8, a), __builtin_bit_cast(bf16x8, b), c, 0, 0, 0);
}
template <int NTP>
__device__ __forceinline__ void dma16(unsigned lds, unsigned voff, __amdgpu_buffer_rsrc_t rs, unsigned soff) {
  if constexpr (NTP == 1)
    asm volatile("s_mov_b32 m0, %0\n\ts_nop 0\n\tbuffer_load_dwordx4 %1, %2, %3 offen nt lds"
                 :: "s"(lds), "v"(voff), "s"(rs), "s"(soff) : "memory");
  else
    asm volatile("s_mov_b32 m0, %0\n\ts_nop 0\n\tbuffer_load_dwordx4 %1, %2, %3 offen lds"
                 :: "s"(lds), "v"(voff), "s"(rs), "s"(soff) : "memory");
}
__device__ __forceinline__ u32x2 trread(LAS unsigned char* p) {
  s16x4 v = __builtin_amdgcn_ds_read_tr16_b64_v4i16((LAS s16x4*)p);
  return __builtin_bit_cast(u32x2, v);
}
__device__ __forceinline__ unsigned wo_src(int lane) {
  const int row = ((lane >> 4) & 1) * 4 + ((lane >> 3) & 1) * 2 + (lane >> 5);
  return (unsigned)row * (DM * 2) + (unsigned)(lane & 7) * 16;
}
template <int STRIDE>
__device__ __forceinline__ void poll4(unsigned (&p)[8], __amdgpu_buffer_rsrc_t rs, unsigned off, unsigned tag) {
  unsigned cap = SPIN_CAP, t0, t1, t2;
#define PL(b) "buffer_load_dwordx4 v[" #b ":" #b "+3], %[off], %[rs], 0 offen sc1\n\t" \
              "buffer_load_dwordx4 v[" #b "+4:" #b "+7], %[off], %[rs], %[s1] offen sc1\n\t" \
              "buffer_load_dwordx4 v[" #b "+8:" #b "+11], %[off], %[rs], %[s2] offen sc1\n\t" \
              "buffer_load_dwordx4 v[" #b "+12:" #b "+15], %[off], %[rs], %[s3] offen sc1\n\t"
#define PC(b) "s_waitcnt vmcnt(4)\n\t" \
              "v_bitop3_b32 %[t0], v[" #b "+1], v[" #b "+3], %[tag] bitop3:0x7e\n\t" \
              "v_bitop3_b32 %[t1], v[" #b "+5], v[" #b "+7], %[tag] bitop3:0x7e\n\t" \
              "v_bitop3_b32 %[t2], v[" #b "+9], v[" #b "+11], %[tag] bitop3:0x7e\n\t" \
              "v_or3_b32 %[t0], %[t0], %[t1], %[t2]\n\t" \
              "v_bitop3_b32 %[t1], v[" #b "+13], v[" #b "+15], %[tag] bitop3:0x7e\n\t" \
              "v_or_b32 %[t0], %[t0], %[t1]\n\t" \
              "v_cmp_ne_u32 vcc, 0, %[t0]\n\t"
#define PM(b) "v_mov_b32 %[p0], v[" #b "]\n\tv_mov_b32 %[p1], v[" #b "+2]\n\tv_mov_b32 %[p2], v[" #b "+4]\n\tv_mov_b32 %[p3], v[" #b "+6]\n\t" \
              "v_mov_b32 %[p4], v[" #b "+8]\n\tv_mov_b32 %[p5], v[" #b "+10]\n\tv_mov_b32 %[p6], v[" #b "+12]\n\tv_mov_b32 %[p7], v[" #b "+14]\n\t"
  asm volatile(
      PL(136)
      "s_sleep %[dl]\n"
      "1:\n\t"
      PL(152)
      PC(136)
      "s_cbranch_vccz 2f\n\t"
      "s_sub_u32 %[cap], %[cap], 1\n\t"
      "s_cbranch_scc1 2f\n\t"
      PL(136)
      PC(152)
      "s_cbranch_vccnz 1b\n\t"
      PM(152)
      "s_branch 3f\n"
      "2:\n\t"
      PM(136)
      "3:\n"
      : [p0] "=&v"(p[0]), [p1] "=&v"(p[1]), [p2] "=&v"(p[2]), [p3] "=&v"(p[3]), [p4] "=&v"(p[4]), [p5] "=&v"(p[5]), [p6] "=&v"(p[6]), [p7] "=&v"(p[7]),
        [t0] "=&v"(t0), [t1] "=&v"(t1), [t2] "=&v"(t2), [cap] "+s"(cap)
      : [off] "v"(off), [rs] "s"(rs), [tag] "v"(tag), [s1] "s"(STRIDE), [s2] "s"(2 * STRIDE), [s3] "s"(3 * STRIDE), [dl] "i"(8)
      : "memory", "vcc", "scc",
        "v136", "v137", "v138", "v139", "v140", "v141", "v142", "v143", "v144", "v145", "v146", "v147", "v148", "v149", "v150", "v151",
        "v152", "v153", "v154", "v155", "v156", "v157", "v158", "v159", "v160", "v161", "v162", "v163", "v164", "v165", "v166", "v167");
#undef PL
#undef PC
#undef PM
}

}

extern "C" __global__ void __launch_bounds__(NT, (NW + NS) / 4)
fused_decode_kernel(const unsigned char* __restrict__ x, const unsigned char* __restrict__ kc,
                    const unsigned char* __restrict__ vc, const unsigned char* __restrict__ wq,
                    const unsigned char* __restrict__ wo, unsigned char* out, unsigned char* ws) {
  __shared__ __attribute__((aligned(16))) unsigned char smem_[L_BYTES];
  LAS unsigned char* smem = (LAS unsigned char*)smem_;
  const unsigned lds0 = (unsigned)(size_t)smem;
  const int tid = threadIdx.x, lane = tid & 63;
  const int wave = __builtin_amdgcn_readfirstlane(tid >> 6);
  const int bid = blockIdx.x, jj = bid >> 3, b = jj & 15, kvh = 2 * ((bid + (b >> 1)) & 7) + (jj >> 4);
  const int li = lane & 15, lg = lane >> 4;
  volatile LAS unsigned* flag = (volatile LAS unsigned*)(smem + L_FLAG);
#define PACE(n) do { __builtin_amdgcn_sched_barrier(0); asm volatile("s_waitcnt vmcnt(%0)" :: "i"((n) - 1) : "memory"); } while (0)

  if (wave >= NW) {
    const int t = wave - NW;
    const __amdgpu_buffer_rsrc_t rs_ws = __builtin_amdgcn_make_buffer_rsrc(ws, 0, WS_BYTES, RSRC_W3);
    if (t == 0) { flag[0] = 0u; flag[1] = 0u; flag[3] = 0u; flag[4] = 0u; flag[5] = 0u; }
    __builtin_amdgcn_s_setprio(3);
    if (t != 0) __syncthreads();
    if (t == 0) {
      if (b >= 8) {
        const __amdgpu_buffer_rsrc_t rs_x = __builtin_amdgcn_make_buffer_rsrc((void*)x, 0, NB * DM * 2, RSRC_W3);
        const unsigned vx = (unsigned)(lane >> 2) * (DM * 2) + (unsigned)(lane & 3) * 16;
#pragma unroll 1
        for (int s = 0; s < 18; ++s) dma16<0>(lds0 + L_X + s * 1024, vx, rs_x, (unsigned)s * 64);
      }
      unsigned epoch = __builtin_amdgcn_raw_buffer_load_b32(rs_ws, WS_EPOCH, 0, AUX_SC1 | AUX_NT);
      __builtin_amdgcn_sched_barrier(0);
      __syncthreads();
      asm volatile("s_waitcnt vmcnt(0)" : "+v"(epoch) :: "memory");
      const unsigned tg = epoch + 1u;
      flag[2] = tg;
      if (b >= 8) {
        while (flag[3] != (unsigned)NW) __builtin_amdgcn_s_sleep(1);
        asm volatile("" ::: "memory");
        f32x4 a0 = {0.f, 0.f, 0.f, 0.f}, a1 = {0.f, 0.f, 0.f, 0.f};
        LAS unsigned char* pw = smem + L_WQ + (8 * lg + ((lane >> 2) & 3)) * 64 + (lane & 3) * 8;
        LAS unsigned char* px = smem + L_X + li * 64 + lg * 16;
#pragma unroll 6
        for (int s = 0; s < 18; ++s) {
          const u32x2 w0 = trread(pw + s * 2048), w1 = trread(pw + s * 2048 + 256);
          const u32x2 w2 = trread(pw + s * 2048 + 32), w3 = trread(pw + s * 2048 + 256 + 32);
          const u32x4 xa = *(LAS u32x4*)(px + s * 1024);
          a0 = mfma16(xa, u32x4{w0[0], w0[1], w1[0], w1[1]}, a0);
          a1 = mfma16(xa, u32x4{w2[0], w2[1], w3[0], w3[1]}, a1);
        }
        LAS unsigned* tq = (LAS unsigned*)(smem + L_OA);
#pragma unroll
        for (int r = 0; r < 4; ++r) tq[(4 * lg + r) * 16 + li] = pkbf(a0[r], a1[r]);
        asm volatile("s_waitcnt lgkmcnt(0)" ::: "memory");
        const unsigned qo = WS_QG + (unsigned)(kvh * 16 + (lane >> 3)) * 1024 + (unsigned)(b - 8) * 128 + (unsigned)(lane & 7) * 16;
#pragma unroll
        for (int k = 0; k < 2; ++k) {
          const u32x2 v = *(LAS u32x2*)(tq + k * 128 + lane * 2);
          __builtin_amdgcn_raw_buffer_store_b128(u32x4{v[0], tg, v[1], tg}, rs_ws, qo, k * 8192, AUX_SC1 | AUX_NT);
        }
        __builtin_amdgcn_s_sleep(32);
      } else {
        __builtin_amdgcn_s_sleep(96);
      }
      const unsigned qi = WS_QG + (unsigned)(kvh * 16 + b) * 1024 + (unsigned)lane * 16;
      u32x4 qr;
      for (unsigned spins = 0;; ++spins) {
        asm volatile("" ::: "memory");
        qr = __builtin_amdgcn_raw_buffer_load_b128(rs_ws, qi, 0, AUX_SC1 | AUX_NT);
        if (__builtin_amdgcn_ballot_w64(((qr[1] ^ tg) | (qr[3] ^ tg)) != 0u) == 0ull || spins > SPIN_CAP) break;
        __builtin_amdgcn_s_sleep(4);
      }
      {
        LAS unsigned* ql = (LAS unsigned*)(smem + L_Q) + 16 * (lane >> 3) + (lane & 7);
        ql[0] = __builtin_amdgcn_perm(qr[2], qr[0], 0x05040100u);
        ql[8] = __builtin_amdgcn_perm(qr[2], qr[0], 0x07060302u);
      }
      asm volatile("s_waitcnt lgkmcnt(0)" ::: "memory");
      flag[0] = 1u;
    } else if (t == 1 && b == 0) {
      const __amdgpu_buffer_rsrc_t rs_wo = __builtin_amdgcn_make_buffer_rsrc((void*)wo, 0, NQ * DM * 2, RSRC_W3);
      const unsigned vwo = wo_src(lane);
#pragma unroll 1
      for (int i = 0; i < 32; ++i) {
        PACE(WOWIN);
        dma16<1>(lds0 + L_WQ + i * 1024, vwo ^ (unsigned)((i & 3) * 32), rs_wo, (unsigned)(kvh * 256 + 8 * i) * (DM * 2) + 8 * 128);
      }
      asm volatile("s_waitcnt vmcnt(0)" ::: "memory");
      flag[4] = 1u;
    }
    while (flag[0] == 0u) __builtin_amdgcn_s_sleep(16);
    asm volatile("" ::: "memory");
    const unsigned tag = flag[2];
    const __amdgpu_buffer_rsrc_t rs_out = __builtin_amdgcn_make_buffer_rsrc(out, 0, NB * DM * 2, RSRC_W3);
    u32x4 wf[8], wf9[8];
    LAS unsigned char* pwf = smem + ((lane >> 2) & 3) * 1024 + lg * 128 + ((t ^ (lane >> 2)) & 3) * 32 + (lane & 3) * 8;
#define LOADWF(dst, base) _Pragma("unroll") for (int s = 0; s < 8; ++s) { const int ro = ((s & 1) * 128 + 32 * (s >> 1)) * 128; \
        const u32x2 w0 = trread(pwf + (base) + ro), w1 = trread(pwf + (base) + ro + 512); dst[s] = u32x4{w0[0], w0[1], w1[0], w1[1]}; }
    if (b == 0) {
      while (flag[4] == 0u) __builtin_amdgcn_s_sleep(16);
      asm volatile("" ::: "memory");
      LOADWF(wf9, L_WQ)
    }
    const int rho = (b - 8) * 16 + kvh, T = rho >> 1, hf = rho & 1;
    unsigned o_pub = WS_OG + (unsigned)(kvh * 16 + b) * 1024 + (unsigned)lane * 16;
    unsigned o_get = b < 8 ? WS_OG + (unsigned)(kvh * 16 + 4 * t) * 1024 + (unsigned)lane * 16
                           : WS_PG + (unsigned)((T * 16 + lg) * 2 + hf) * 1024 + (unsigned)(16 * t + li) * 16;
    unsigned o_put = b < 8 ? WS_PG + (unsigned)(((4 * b + t) * 16 + kvh) * 2) * 1024 + (unsigned)lane * 16
                           : (lg == 0 ? (unsigned)(li * DM + 16 * T + 4 * t + 2 * hf) * 2 : OOB);
    asm volatile("" : "+v"(o_pub), "+v"(o_get), "+v"(o_put));
    if (b < 8 && t > 0) {
      const __amdgpu_buffer_rsrc_t rs_wo = __builtin_amdgcn_make_buffer_rsrc((void*)wo, 0, NQ * DM * 2, RSRC_W3);
      const unsigned vwo = wo_src(lane);
      while (flag[5] != (unsigned)NW) __builtin_amdgcn_s_sleep(2);
#pragma unroll 1
      for (int i = t - 1; i < 32; i += 3)
        dma16<1>(lds0 + L_WO + i * 1024, vwo ^ (unsigned)((i & 3) * 32), rs_wo, (unsigned)(kvh * 256 + 8 * i) * (DM * 2) + (unsigned)b * 128);
    }
    __syncthreads();
    if (t == 0) {
      f32x4 pr[NW];
#pragma unroll
      for (int w = 0; w < NW; ++w) pr[w] = *(LAS f32x4*)(smem + L_RED + w * 1024 + lane * 16);
      __builtin_amdgcn_sched_barrier(0);
#pragma unroll
      for (int s = 1; s < NW; s *= 2)
#pragma unroll
        for (int w = 0; w < NW; w += 2 * s) pr[w] += pr[w + s];
      const f32x4 o = pr[0];
      __builtin_amdgcn_raw_buffer_store_b128(u32x4{pkbf(o[0], o[1]), tag, pkbf(o[2], o[3]), tag}, rs_ws, o_pub, 0, AUX_SC1);
    }
    unsigned p[8];
    if (b < 8) {
      if (t > 0) {
        asm volatile("s_waitcnt vmcnt(0)" ::: "memory");
        if (lane == 0) __hip_atomic_fetch_add((LAS unsigned*)(smem + L_FLAG + 12), 1u, __ATOMIC_RELAXED, __HIP_MEMORY_SCOPE_WORKGROUP);
      }
      while (flag[3] != 3u) __builtin_amdgcn_s_sleep(1);
      asm volatile("" ::: "memory");
      LOADWF(wf, L_WO)
      __builtin_amdgcn_sched_barrier(0);
      poll4<1024>(p, rs_ws, o_get, tag);
#pragma unroll
      for (int i = 0; i < 4; ++i) *(LAS u32x2*)(smem + L_OA + (4 * t + i) * 528 + lane * 8) = u32x2{p[2 * i], p[2 * i + 1]};
      __syncthreads();
      u32x4 ob[8];
#pragma unroll
      for (int s = 0; s < 8; ++s) ob[s] = *(LAS u32x4*)(smem + L_OA + li * 528 + s * 64 + lg * 16);
      __builtin_amdgcn_sched_barrier(0);
      f32x4 d = {0.f, 0.f, 0.f, 0.f};
#pragma unroll
      for (int s = 0; s < 8; ++s) d = mfma16(wf[s], ob[s], d);
      __builtin_amdgcn_raw_buffer_store_b128(u32x4{__float_as_uint(d[0]), tag, __float_as_uint(d[1]), tag}, rs_ws, o_put, 0, AUX_SC1);
      __builtin_amdgcn_raw_buffer_store_b128(u32x4{__float_as_uint(d[2]), tag, __float_as_uint(d[3]), tag}, rs_ws, o_put, 1024, AUX_SC1);
      if (b == 0) {
        f32x4 e = {0.f, 0.f, 0.f, 0.f};
#pragma unroll
        for (int s = 0; s < 8; ++s) e = mfma16(wf9[s], ob[s], e);
        __builtin_amdgcn_raw_buffer_store_b128(u32x4{__float_as_uint(e[0]), tag, __float_as_uint(e[1]), tag}, rs_ws, o_put, 32 * 32768, AUX_SC1);
        __builtin_amdgcn_raw_buffer_store_b128(u32x4{__float_as_uint(e[2]), tag, __float_as_uint(e[3]), tag}, rs_ws, o_put, 32 * 32768 + 1024, AUX_SC1);
      }
    } else if (rho < 72) {
      poll4<4 * 2048>(p, rs_ws, o_get, tag);
      float s0 = (__uint_as_float(p[0]) + __uint_as_float(p[2])) + (__uint_as_float(p[4]) + __uint_as_float(p[6]));
      float s1 = (__uint_as_float(p[1]) + __uint_as_float(p[3])) + (__uint_as_float(p[5]) + __uint_as_float(p[7]));
      s0 = xrow_sum(s0); s1 = xrow_sum(s1);
      __builtin_amdgcn_raw_buffer_store_b32(pkbf(s0, s1), rs_out, o_put, 0, 0);
      if (rho == 0 && t == 0) __builtin_amdgcn_raw_buffer_store_b32(tag, rs_ws, WS_EPOCH, 0, AUX_SC1);
    }
    asm volatile("s_waitcnt vmcnt(0)" ::: "memory");
    return;
  }

  const int sw = wave;
  const unsigned tb = (unsigned)__builtin_amdgcn_s_memrealtime();
  const bool isq = b >= 8;
  if (isq) {
    const __amdgpu_buffer_rsrc_t rs_w = __builtin_amdgcn_make_buffer_rsrc((void*)wq, 0, DM * NQ * 2, RSRC_W3);
    const unsigned vw = (unsigned)(lane >> 2) * (NQ * 2) + (unsigned)(lane & 3) * 16 + (unsigned)(kvh * 256 + 32 * (b - 8)) * 2;
    int cnt = 0;
#pragma unroll 1
    for (int i = sw; i < 36; i += NW) {
      if (cnt >= KWIN) PACE(KWIN);
      dma16<1>(lds0 + L_WQ + i * 1024, vw, rs_w, (unsigned)i * (16 * NQ * 2));
      if (++cnt == KWIN) __syncthreads();
    }
    static_assert(KWIN <= 4, "every streaming wave must reach the barrier inside the weight loop");
  }
  const size_t kvoff = ((size_t)(b * HKV + kvh) * SK + (SK - WIN)) * (DH * 2);
  const __amdgpu_buffer_rsrc_t rs_k = __builtin_amdgcn_make_buffer_rsrc((void*)(kc + kvoff), 0, WIN * DH * 2, RSRC_W3);
  const __amdgpu_buffer_rsrc_t rs_v = __builtin_amdgcn_make_buffer_rsrc((void*)(vc + kvoff), 0, WIN * DH * 2, RSRC_W3);
  const unsigned voff = (unsigned)lane * 16 + (unsigned)sw * (NL * 1024);
  const unsigned ph = (unsigned)sw * 4096;
#define SOFF(u) (((u) * 1024 + ph) & (NL * 1024 - 1))
  u32x4 K[NL], V[NL], qA, qB;
  float y[NL], m = -INFINITY, mc = 0.f, l = 0.f, f = 0.f;
  const int hA = li >> 3;
  constexpr float C2 = 0.088388347648318440f * 1.4426950408889634f;
  const unsigned hm = (lane & 1) ? 0xffff0000u : 0x0000ffffu;
  const int rsel = (lane & 7) >> 1;
  const unsigned mk0 = rsel == 0 ? hm : 0u, mk1 = rsel == 1 ? hm : 0u, mk2 = rsel == 2 ? hm : 0u, mk3 = rsel == 3 ? hm : 0u;
  f32x4 acc = {0.f, 0.f, 0.f, 0.f};
  unsigned tnow = tb;
#define PV(u) do { const unsigned pp = pkbf(y[u], y[u]); acc = mfma16(V[u], u32x4{pp & mk0, pp & mk1, pp & mk2, pp & mk3}, acc); } while (0)
  sfor<2 * NL>([&](auto ic) __attribute__((always_inline)) {
    constexpr int i = decltype(ic)::value;
    if constexpr (i < KWIN) PACE(KWIN);
    else {
      PACE(VWIN);
      if (tnow - tb < (unsigned)((i - 1 + TOFF) * SLOT_NS / 10)) PACE(KWIN);
      __builtin_amdgcn_sched_barrier(0);
    }
    if constexpr (i == KWIN - 1) {
      if (isq && lane == 0) __hip_atomic_fetch_add((LAS unsigned*)(smem + L_FLAG + 12), 1u, __ATOMIC_RELAXED, __HIP_MEMORY_SCOPE_WORKGROUP);
    }
    if constexpr (i < NL) K[i] = __builtin_amdgcn_raw_buffer_load_b128(rs_k, voff, SOFF(i), AUX_NT);
    else V[i - NL] = __builtin_amdgcn_raw_buffer_load_b128(rs_v, voff, SOFF(i - NL), AUX_NT);
    __builtin_amdgcn_sched_barrier(0);
    tnow = (unsigned)__builtin_amdgcn_s_memrealtime();
    __builtin_amdgcn_sched_barrier(0);
    if constexpr (i == KWIN - 1) {
      if (!isq) __syncthreads();
    }
    if constexpr (i == 2 * NL - 1) {
      if (lane == 0) __hip_atomic_fetch_add((LAS unsigned*)(smem + L_FLAG + 20), 1u, __ATOMIC_RELAXED, __HIP_MEMORY_SCOPE_WORKGROUP);
      __builtin_amdgcn_sched_barrier(0);
    }
    if constexpr (i == QS) {
      while (flag[0] == 0u) __builtin_amdgcn_s_sleep(1);
      asm volatile("" ::: "memory");
      qA = *(LAS u32x4*)(smem + L_Q + hA * 256 + li * 16);
      qB = *(LAS u32x4*)(smem + L_Q + (hA ^ 1) * 256 + li * 16);
    }
    if constexpr (i >= QS && i < ES) {
#pragma unroll
      for (int u = 2 * (i - QS); u < 2 * (i - QS) + 2; ++u) {
        float xa = 0.f, xb = 0.f;
#pragma unroll
        for (int k = 0; k < 4; ++k) { xa = dot2(K[u][k], qA[k], xa); xb = dot2(K[u][k], qB[k], xb); }
        float t = xa + dpp<ROR8>(xb);
        t += dpp<XOR1>(t);
        t += dpp<XOR2>(t);
        t += dpp<HMIRROR>(t);
        y[u] = t;
        m = fmaxf(m, t);
        asm volatile("" : "+v"(y[u]), "+v"(m));
      }
    }
    if constexpr (i == ES) mc = xrow_max(m) * C2;
    if constexpr (i >= ES && i < PS) {
#pragma unroll
      for (int u = (NL / 4) * (i - ES); u < (NL / 4) * (i - ES + 1); ++u) { y[u] = __builtin_amdgcn_exp2f(fmaf(y[u], C2, -mc)); l += y[u]; }
      asm volatile("" : "+v"(l));
    }
    if constexpr (i == PS - 1) {
      l = xrow_sum(l);
      *(LAS f32x2*)(smem + L_ML + hA * (NW * 8) + sw * 8) = f32x2{mc, l};
      asm volatile("s_waitcnt lgkmcnt(0)" ::: "memory");
      if (lane == 0) __hip_atomic_fetch_add((LAS unsigned*)(smem + L_FLAG + 4), 1u, __ATOMIC_RELAXED, __HIP_MEMORY_SCOPE_WORKGROUP);
    }
    if constexpr (i >= PS) {
#pragma unroll
      for (int u = npv_after(i - 1); u < npv_after(i); ++u) PV(u);
      asm volatile("" : "+v"(acc));
    }
    if constexpr (i == FIN_AT) {
      __builtin_amdgcn_sched_barrier(0);
      while (flag[1] != (unsigned)NW) __builtin_amdgcn_s_sleep(1);
      asm volatile("" ::: "memory");
      f32x4 ml[NW / 2];
#pragma unroll
      for (int w = 0; w < NW / 2; ++w) ml[w] = *(LAS f32x4*)(smem + L_ML + hA * (NW * 8) + w * 16);
      float M = fmaxf(ml[0][0], ml[0][2]);
#pragma unroll
      for (int w = 1; w < NW / 2; ++w) M = fmaxf(M, fmaxf(ml[w][0], ml[w][2]));
      float L = 0.f;
#pragma unroll
      for (int w = 0; w < NW / 2; ++w) {
        L = fmaf(ml[w][1], __builtin_amdgcn_exp2f(ml[w][0] - M), L);
        L = fmaf(ml[w][3], __builtin_amdgcn_exp2f(ml[w][2] - M), L);
      }
      f = __builtin_amdgcn_exp2f(mc - M) / L;
      asm volatile("" : "+v"(f));
    }
  });
  __builtin_amdgcn_sched_barrier(0);
#pragma unroll
  for (int u = npv_after(2 * NL - 1); u < NL; ++u) PV(u);
  *(LAS f32x4*)(smem + L_RED + sw * 1024 + lane * 16) = acc * f;
  __syncthreads();
}

extern "C" void kernel_launch(void* const* d_in, const int* in_sizes, int n_in, void* d_out, int out_size,
                              void* d_ws, size_t ws_size, hipStream_t stream) {
  fused_decode_kernel<<<256, NT, 0, stream>>>((const unsigned char*)d_in[0], (const unsigned char*)d_in[1],
                                             (const unsigned char*)d_in[2], (const unsigned char*)d_in[3],
                                             (const unsigned char*)d_in[4], (unsigned char*)d_out,
                                             (unsigned char*)d_ws);
}
```

```cpp
#include <hip/hip_runtime.h>
#include <hip/hip_bf16.h>
#include <stdint.h>
#include <utility>

#define LAS __attribute__((address_space(3)))
typedef unsigned u32x4 __attribute__((ext_vector_type(4)));
typedef unsigned u32x2 __attribute__((ext_vector_type(2)));
typedef float f32x4 __attribute__((ext_vector_type(4)));
typedef float f32x2 __attribute__((ext_vector_type(2)));
typedef short s16x4 __attribute__((ext_vector_type(4)));
typedef __bf16 bf16x8 __attribute__((ext_vector_type(8)));
typedef __bf16 bf16x2 __attribute__((ext_vector_type(2)));

namespace {

constexpr int NB = 16, DM = 576, HKV = 16, SK = 4096, DH = 128, WIN = 1024, NQ = 4096;
constexpr int NW = 8, NS = 4, NT = (NW + NS) * 64;
constexpr int NL = 256 / NW;

constexpr int KWIN = 3, VWIN = 5;
constexpr int SLOT_NS = 283, TOFF = 4;
constexpr int QS = 26;
constexpr int FIN_AT = 58;
constexpr int AUX_NT = 2, AUX_SC1 = 16;
constexpr int RSRC_W3 = 0x00020000;
constexpr unsigned OOB = 0x7ffffff0u;

constexpr unsigned WS_EPOCH = 0;
constexpr unsigned WS_QG = 4096;
constexpr unsigned WS_OG = WS_QG + 256 * 1024;
constexpr unsigned WS_PG = WS_OG + 256 * 1024;
constexpr unsigned WS_BYTES = WS_PG + 36 * 16 * 2 * 1024;

constexpr int L_WQ = 0;
constexpr int L_X = L_WQ + 36864;
constexpr int L_WO = L_X + 18432;
constexpr int L_RED = L_WO + 32768;
constexpr int L_OA = L_RED + NW * 1024;
constexpr int L_Q = L_OA + 16 * 528;
constexpr int L_ML = L_Q + 512;
constexpr int L_FLAG = L_ML + NW * 16;
constexpr int L_BYTES = L_FLAG + 32;

constexpr unsigned SPIN_CAP = 1u << 18;

template <class F, int... I>
__device__ __forceinline__ void sfor_impl(F&& f, std::integer_sequence<int, I...>) { (f(std::integral_constant<int, I>{}), ...); }
template <int N, class F>
__device__ __forceinline__ void sfor(F&& f) { sfor_impl(f, std::make_integer_sequence<int, N>{}); }
constexpr int ES = QS + NL / 2, PS = ES + 4;
constexpr int npv_after(int slot) {
  int n = 0;
  for (int i = PS; i <= slot; ++i)
    for (int k = 0; k < 2; ++k)
      if (n <= i - NL - VWIN) ++n;
  return n;
}

__device__ __forceinline__ float dot2(unsigned k, unsigned q, float acc) {
  return __builtin_amdgcn_fdot2_f32_bf16(__builtin_bit_cast(bf16x2, k), __builtin_bit_cast(bf16x2, q), acc, false);
}
__device__ __forceinline__ unsigned pkbf(float lo, float hi) {
  f32x2 f = {lo, hi};
  return __builtin_bit_cast(unsigned, __builtin_convertvector(f, bf16x2));
}
template <int CTRL> __device__ __forceinline__ float dpp(float x) {
  return __builtin_bit_cast(float, __builtin_amdgcn_mov_dpp(__builtin_bit_cast(int, x), CTRL, 0xf, 0xf, true));
}
constexpr int XOR1 = 0xB1, XOR2 = 0x4E, HMIRROR = 0x141, ROR8 = 0x128;
__device__ __forceinline__ float xrow_max(float x) {
  auto s = __builtin_amdgcn_permlane16_swap(__float_as_uint(x), __float_as_uint(x), false, false);
  x = fmaxf(__uint_as_float(s[0]), __uint_as_float(s[1]));
  auto t = __builtin_amdgcn_permlane32_swap(__float_as_uint(x), __float_as_uint(x), false, false);
  return fmaxf(__uint_as_float(t[0]), __uint_as_float(t[1]));
}
__device__ __forceinline__ float xrow_sum(float x) {
  auto s = __builtin_amdgcn_permlane16_swap(__float_as_uint(x), __float_as_uint(x), false, false);
  x = __uint_as_float(s[0]) + __uint_as_float(s[1]);
  auto t = __builtin_amdgcn_permlane32_swap(__float_as_uint(x), __float_as_uint(x), false, false);
  return __uint_as_float(t[0]) + __uint_as_float(t[1]);
}
__device__ __forceinline__ f32x4 mfma16(u32x4 a, u32x4 b, f32x4 c) {
  return __builtin_amdgcn_mfma_f32_16x16x32_bf16(__builtin_bit_cast(bf16x8, a), __builtin_bit_cast(bf16x8, b), c, 0, 0, 0);
}
template <int NTP>
__device__ __forceinline__ void dma16(unsigned lds, unsigned voff, __amdgpu_buffer_rsrc_t rs, unsigned soff) {
  if constexpr (NTP == 1)
    asm volatile("s_mov_b32 m0, %0\n\ts_nop 0\n\tbuffer_load_dwordx4 %1, %2, %3 offen nt lds"
                 :: "s"(lds), "v"(voff), "s"(rs), "s"(soff) : "memory");
  else
    asm volatile("s_mov_b32 m0, %0\n\ts_nop 0\n\tbuffer_load_dwordx4 %1, %2, %3 offen lds"
                 :: "s"(lds), "v"(voff), "s"(rs), "s"(soff) : "memory");
}
__device__ __forceinline__ u32x2 trread(LAS unsigned char* p) {
  s16x4 v = __builtin_amdgcn_ds_read_tr16_b64_v4i16((LAS s16x4*)p);
  return __builtin_bit_cast(u32x2, v);
}
__device__ __forceinline__ unsigned wo_src(int lane) {
  const int row = ((lane >> 4) & 1) * 4 + ((lane >> 3) & 1) * 2 + (lane >> 5);
  return (unsigned)row * (DM * 2) + (unsigned)(lane & 7) * 16;
}
template <int STRIDE>
__device__ __forceinline__ void poll4(unsigned (&p)[8], __amdgpu_buffer_rsrc_t rs, unsigned off, unsigned tag) {
  unsigned cap = SPIN_CAP, t0, t1, t2;
#define PL(b) "buffer_load_dwordx4 v[" #b ":" #b "+3], %[off], %[rs], 0 offen sc1\n\t" \
              "buffer_load_dwordx4 v[" #b "+4:" #b "+7], %[off], %[rs], %[s1] offen sc1\n\t" \
              "buffer_load_dwordx4 v[" #b "+8:" #b "+11], %[off], %[rs], %[s2] offen sc1\n\t" \
              "buffer_load_dwordx4 v[" #b "+12:" #b "+15], %[off], %[rs], %[s3] offen sc1\n\t"
#define PC(b) "s_waitcnt vmcnt(4)\n\t" \
              "v_bitop3_b32 %[t0], v[" #b "+1], v[" #b "+3], %[tag] bitop3:0x7e\n\t" \
              "v_bitop3_b32 %[t1], v[" #b "+5], v[" #b "+7], %[tag] bitop3:0x7e\n\t" \
              "v_bitop3_b32 %[t2], v[" #b "+9], v[" #b "+11], %[tag] bitop3:0x7e\n\t" \
              "v_or3_b32 %[t0], %[t0], %[t1], %[t2]\n\t" \
              "v_bitop3_b32 %[t1], v[" #b "+13], v[" #b "+15], %[tag] bitop3:0x7e\n\t" \
              "v_or_b32 %[t0], %[t0], %[t1]\n\t" \
              "v_cmp_ne_u32 vcc, 0, %[t0]\n\t"
#define PM(b) "v_mov_b32 %[p0], v[" #b "]\n\tv_mov_b32 %[p1], v[" #b "+2]\n\tv_mov_b32 %[p2], v[" #b "+4]\n\tv_mov_b32 %[p3], v[" #b "+6]\n\t" \
              "v_mov_b32 %[p4], v[" #b "+8]\n\tv_mov_b32 %[p5], v[" #b "+10]\n\tv_mov_b32 %[p6], v[" #b "+12]\n\tv_mov_b32 %[p7], v[" #b "+14]\n\t"
  asm volatile(
      PL(136)
      "s_sleep %[dl]\n"
      "1:\n\t"
      PL(152)
      PC(136)
      "s_cbranch_vccz 2f\n\t"
      "s_sub_u32 %[cap], %[cap], 1\n\t"
      "s_cbranch_scc1 2f\n\t"
      PL(136)
      PC(152)
      "s_cbranch_vccnz 1b\n\t"
      PM(152)
      "s_branch 3f\n"
      "2:\n\t"
      PM(136)
      "3:\n"
      : [p0] "=&v"(p[0]), [p1] "=&v"(p[1]), [p2] "=&v"(p[2]), [p3] "=&v"(p[3]), [p4] "=&v"(p[4]), [p5] "=&v"(p[5]), [p6] "=&v"(p[6]), [p7] "=&v"(p[7]),
        [t0] "=&v"(t0), [t1] "=&v"(t1), [t2] "=&v"(t2), [cap] "+s"(cap)
      : [off] "v"(off), [rs] "s"(rs), [tag] "v"(tag), [s1] "s"(STRIDE), [s2] "s"(2 * STRIDE), [s3] "s"(3 * STRIDE), [dl] "i"(8)
      : "memory", "vcc", "scc",
        "v136", "v137", "v138", "v139", "v140", "v141", "v142", "v143", "v144", "v145", "v146", "v147", "v148", "v149", "v150", "v151",
        "v152", "v153", "v154", "v155", "v156", "v157", "v158", "v159", "v160", "v161", "v162", "v163", "v164", "v165", "v166", "v167");
#undef PL
#undef PC
#undef PM
}

}

extern "C" __global__ void __launch_bounds__(NT, (NW + NS) / 4)
fused_decode_kernel(const unsigned char* __restrict__ x, const unsigned char* __restrict__ kc,
                    const unsigned char* __restrict__ vc, const unsigned char* __restrict__ wq,
                    const unsigned char* __restrict__ wo, unsigned char* out, unsigned char* ws) {
  __shared__ __attribute__((aligned(16))) unsigned char smem_[L_BYTES];
  LAS unsigned char* smem = (LAS unsigned char*)smem_;
  const unsigned lds0 = (unsigned)(size_t)smem;
  const int tid = threadIdx.x, lane = tid & 63;
  const int wave = __builtin_amdgcn_readfirstlane(tid >> 6);
  const int bid = blockIdx.x, jj = bid >> 3, b = jj & 15, kvh = 2 * ((bid + (b >> 1)) & 7) + (jj >> 4);
  const int li = lane & 15, lg = lane >> 4;
  volatile LAS unsigned* flag = (volatile LAS unsigned*)(smem + L_FLAG);
#define PACE(n) do { __builtin_amdgcn_sched_barrier(0); asm volatile("s_waitcnt vmcnt(%0)" :: "i"((n) - 1) : "memory"); } while (0)

  if (wave >= NW) {
    const int t = wave - NW;
    const __amdgpu_buffer_rsrc_t rs_ws = __builtin_amdgcn_make_buffer_rsrc(ws, 0, WS_BYTES, RSRC_W3);
    if (t == 0) { flag[0] = 0u; flag[1] = 0u; flag[3] = 0u; flag[4] = 0u; flag[5] = 0u; }
    __builtin_amdgcn_s_setprio(3);
    if (t != 0) __syncthreads();
    if (t == 0) {
      if (b >= 8) {
        const __amdgpu_buffer_rsrc_t rs_x = __builtin_amdgcn_make_buffer_rsrc((void*)x, 0, NB * DM * 2, RSRC_W3);
        const unsigned vx = (unsigned)(lane >> 2) * (DM * 2) + (unsigned)(lane & 3) * 16;
#pragma unroll 1
        for (int s = 0; s < 18; ++s) dma16<0>(lds0 + L_X + s * 1024, vx, rs_x, (unsigned)s * 64);
      }
      unsigned epoch = __builtin_amdgcn_raw_buffer_load_b32(rs_ws, WS_EPOCH, 0, AUX_SC1 | AUX_NT);
      __builtin_amdgcn_sched_barrier(0);
      __syncthreads();
      asm volatile("s_waitcnt vmcnt(0)" : "+v"(epoch) :: "memory");
      const unsigned tg = epoch + 1u;
      flag[2] = tg;
      if (b >= 8) {
        while (flag[3] != (unsigned)NW) __builtin_amdgcn_s_sleep(1);
        asm volatile("" ::: "memory");
        f32x4 a0 = {0.f, 0.f, 0.f, 0.f}, a1 = {0.f, 0.f, 0.f, 0.f};
        LAS unsigned char* pw = smem + L_WQ + (8 * lg + ((lane >> 2) & 3)) * 64 + (lane & 3) * 8;
        LAS unsigned char* px = smem + L_X + li * 64 + lg * 16;
#pragma unroll 6
        for (int s = 0; s < 18; ++s) {
          const u32x2 w0 = trread(pw + s * 2048), w1 = trread(pw + s * 2048 + 256);
          const u32x2 w2 = trread(pw + s * 2048 + 32), w3 = trread(pw + s * 2048 + 256 + 32);
          const u32x4 xa = *(LAS u32x4*)(px + s * 1024);
          a0 = mfma16(xa, u32x4{w0[0], w0[1], w1[0], w1[1]}, a0);
          a1 = mfma16(xa, u32x4{w2[0], w2[1], w3[0], w3[1]}, a1);
        }
        LAS unsigned* tq = (LAS unsigned*)(smem + L_OA);
#pragma unroll
        for (int r = 0; r < 4; ++r) tq[(4 * lg + r) * 16 + li] = pkbf(a0[r], a1[r]);
        asm volatile("s_waitcnt lgkmcnt(0)" ::: "memory");
        const unsigned qo = WS_QG + (unsigned)(kvh * 16 + (lane >> 3)) * 1024 + (unsigned)(b - 8) * 128 + (unsigned)(lane & 7) * 16;
#pragma unroll
        for (int k = 0; k < 2; ++k) {
          const u32x2 v = *(LAS u32x2*)(tq + k * 128 + lane * 2);
          __builtin_amdgcn_raw_buffer_store_b128(u32x4{v[0], tg, v[1], tg}, rs_ws, qo, k * 8192, AUX_SC1 | AUX_NT);
        }
        __builtin_amdgcn_s_sleep(32);
      } else {
        __builtin_amdgcn_s_sleep(96);
      }
      const unsigned qi = WS_QG + (unsigned)(kvh * 16 + b) * 1024 + (unsigned)lane * 16;
      u32x4 qr;
      for (unsigned spins = 0;; ++spins) {
        asm volatile("" ::: "memory");
        qr = __builtin_amdgcn_raw_buffer_load_b128(rs_ws, qi, 0, AUX_SC1 | AUX_NT);
        if (__builtin_amdgcn_ballot_w64(((qr[1] ^ tg) | (qr[3] ^ tg)) != 0u) == 0ull || spins > SPIN_CAP) break;
        __builtin_amdgcn_s_sleep(4);
      }
      {
        LAS unsigned* ql = (LAS unsigned*)(smem + L_Q) + 16 * (lane >> 3) + (lane & 7);
        ql[0] = __builtin_amdgcn_perm(qr[2], qr[0], 0x05040100u);
        ql[8] = __builtin_amdgcn_perm(qr[2], qr[0], 0x07060302u);
      }
      asm volatile("s_waitcnt lgkmcnt(0)" ::: "memory");
      flag[0] = 1u;
    }
    while (flag[0] == 0u) __builtin_amdgcn_s_sleep(16);
    asm volatile("" ::: "memory");
    const unsigned tag = flag[2];
    const __amdgpu_buffer_rsrc_t rs_out = __builtin_amdgcn_make_buffer_rsrc(out, 0, NB * DM * 2, RSRC_W3);
    u32x4 wf[8];
    LAS unsigned char* pwf = smem + ((lane >> 2) & 3) * 1024 + lg * 128 + ((t ^ (lane >> 2)) & 3) * 32 + (lane & 3) * 8;
#define LOADWF(dst, base) _Pragma("unroll") for (int s = 0; s < 8; ++s) { const int ro = ((s & 1) * 128 + 32 * (s >> 1)) * 128; \
        const u32x2 w0 = trread(pwf + (base) + ro), w1 = trread(pwf + (base) + ro + 512); dst[s] = u32x4{w0[0], w0[1], w1[0], w1[1]}; }
    const bool st1 = b < 8 || b == 13;
    const int cs = b < 8 ? b : 8;
    const int rho = (b - 8) * 16 + kvh, T = rho >> 1, hf = rho & 1;
    unsigned o_pub = WS_OG + (unsigned)(kvh * 16 + b) * 1024 + (unsigned)lane * 16;
    unsigned o_get = st1 ? WS_OG + (unsigned)(kvh * 16 + 4 * t) * 1024 + (unsigned)lane * 16
                         : WS_PG + (unsigned)((T * 16 + lg) * 2 + hf) * 1024 + (unsigned)(16 * t + li) * 16;
    unsigned o_put = st1 ? WS_PG + (unsigned)(((4 * cs + t) * 16 + kvh) * 2) * 1024 + (unsigned)lane * 16
                         : (lg == 0 ? (unsigned)(li * DM + 16 * T + 4 * t + 2 * hf) * 2 : OOB);
    asm volatile("" : "+v"(o_pub), "+v"(o_get), "+v"(o_put));
    if (st1 && t > 0) {
      const __amdgpu_buffer_rsrc_t rs_wo = __builtin_amdgcn_make_buffer_rsrc((void*)wo, 0, NQ * DM * 2, RSRC_W3);
      const unsigned vwo = wo_src(lane);
      while (flag[5] != (unsigned)NW) __builtin_amdgcn_s_sleep(2);
#pragma unroll 1
      for (int i = t - 1; i < 32; i += 3)
        dma16<1>(lds0 + L_WO + i * 1024, vwo ^ (unsigned)((i & 3) * 32), rs_wo, (unsigned)(kvh * 256 + 8 * i) * (DM * 2) + (unsigned)cs * 128);
    }
    __syncthreads();
    if (t == 0) {
      f32x4 pr[NW];
#pragma unroll
      for (int w = 0; w < NW; ++w) pr[w] = *(LAS f32x4*)(smem + L_RED + w * 1024 + lane * 16);
      __builtin_amdgcn_sched_barrier(0);
#pragma unroll
      for (int s = 1; s < NW; s *= 2)
#pragma unroll
        for (int w = 0; w < NW; w += 2 * s) pr[w] += pr[w + s];
      const f32x4 o = pr[0];
      __builtin_amdgcn_raw_buffer_store_b128(u32x4{pkbf(o[0], o[1]), tag, pkbf(o[2], o[3]), tag}, rs_ws, o_pub, 0, AUX_SC1);
    }
    unsigned p[8];
    if (st1) {
      if (t > 0) {
        asm volatile("s_waitcnt vmcnt(0)" ::: "memory");
        if (lane == 0) __hip_atomic_fetch_add((LAS unsigned*)(smem + L_FLAG + 16), 1u, __ATOMIC_RELAXED, __HIP_MEMORY_SCOPE_WORKGROUP);
      }
      while (flag[4] != 3u) __builtin_amdgcn_s_sleep(1);
      asm volatile("" ::: "memory");
      LOADWF(wf, L_WO)
      __builtin_amdgcn_sched_barrier(0);
      poll4<1024>(p, rs_ws, o_get, tag);
#pragma unroll
      for (int i = 0; i < 4; ++i) *(LAS u32x2*)(smem + L_OA + (4 * t + i) * 528 + lane * 8) = u32x2{p[2 * i], p[2 * i + 1]};
      __syncthreads();
      u32x4 ob[8];
#pragma unroll
      for (int s = 0; s < 8; ++s) ob[s] = *(LAS u32x4*)(smem + L_OA + li * 528 + s * 64 + lg * 16);
      __builtin_amdgcn_sched_barrier(0);
      f32x4 d = {0.f, 0.f, 0.f, 0.f};
#pragma unroll
      for (int s = 0; s < 8; ++s) d = mfma16(wf[s], ob[s], d);
      __builtin_amdgcn_raw_buffer_store_b128(u32x4{__float_as_uint(d[0]), tag, __float_as_uint(d[1]), tag}, rs_ws, o_put, 0, AUX_SC1);
      __builtin_amdgcn_raw_buffer_store_b128(u32x4{__float_as_uint(d[2]), tag, __float_as_uint(d[3]), tag}, rs_ws, o_put, 1024, AUX_SC1);
    } else if (rho < 72) {
      poll4<4 * 2048>(p, rs_ws, o_get, tag);
      float s0 = (__uint_as_float(p[0]) + __uint_as_float(p[2])) + (__uint_as_float(p[4]) + __uint_as_float(p[6]));
      float s1 = (__uint_as_float(p[1]) + __uint_as_float(p[3])) + (__uint_as_float(p[5]) + __uint_as_float(p[7]));
      s0 = xrow_sum(s0); s1 = xrow_sum(s1);
      __builtin_amdgcn_raw_buffer_store_b32(pkbf(s0, s1), rs_out, o_put, 0, 0);
      if (rho == 0 && t == 0) __builtin_amdgcn_raw_buffer_store_b32(tag, rs_ws, WS_EPOCH, 0, AUX_SC1);
    }
    asm volatile("s_waitcnt vmcnt(0)" ::: "memory");
    return;
  }

  const int sw = wave;
  const unsigned tb = (unsigned)__builtin_amdgcn_s_memrealtime();
  const bool isq = b >= 8;
  if (isq) {
    const __amdgpu_buffer_rsrc_t rs_w = __builtin_amdgcn_make_buffer_rsrc((void*)wq, 0, DM * NQ * 2, RSRC_W3);
    const unsigned vw = (unsigned)(lane >> 2) * (NQ * 2) + (unsigned)(lane & 3) * 16 + (unsigned)(kvh * 256 + 32 * (b - 8)) * 2;
    int cnt = 0;
#pragma unroll 1
    for (int i = sw; i < 36; i += NW) {
      if (cnt >= KWIN) PACE(KWIN);
      dma16<1>(lds0 + L_WQ + i * 1024, vw, rs_w, (unsigned)i * (16 * NQ * 2));
      if (++cnt == KWIN) __syncthreads();
    }
    static_assert(KWIN <= 4, "every streaming wave must reach the barrier inside the weight loop");
  }
  const size_t kvoff = ((size_t)(b * HKV + kvh) * SK + (SK - WIN)) * (DH * 2);
  const __amdgpu_buffer_rsrc_t rs_k = __builtin_amdgcn_make_buffer_rsrc((void*)(kc + kvoff), 0, WIN * DH * 2, RSRC_W3);
  const __amdgpu_buffer_rsrc_t rs_v = __builtin_amdgcn_make_buffer_rsrc((void*)(vc + kvoff), 0, WIN * DH * 2, RSRC_W3);
  const unsigned voff = (unsigned)lane * 16 + (unsigned)sw * (NL * 1024);
  const unsigned ph = (unsigned)sw * 4096;
#define SOFF(u) (((u) * 1024 + ph) & (NL * 1024 - 1))
  u32x4 K[NL], V[NL], qA, qB;
  float y[NL], m = -INFINITY, mc = 0.f, l = 0.f, f = 0.f;
  const int hA = li >> 3;
  constexpr float C2 = 0.088388347648318440f * 1.4426950408889634f;
  const unsigned hm = (lane & 1) ? 0xffff0000u : 0x0000ffffu;
  const int rsel = (lane & 7) >> 1;
  const unsigned mk0 = rsel == 0 ? hm : 0u, mk1 = rsel == 1 ? hm : 0u, mk2 = rsel == 2 ? hm : 0u, mk3 = rsel == 3 ? hm : 0u;
  f32x4 acc = {0.f, 0.f, 0.f, 0.f};
  unsigned tnow = tb;
#define PV(u) do { const unsigned pp = pkbf(y[u], y[u]); acc = mfma16(V[u], u32x4{pp & mk0, pp & mk1, pp & mk2, pp & mk3}, acc); } while (0)
  sfor<2 * NL>([&](auto ic) __attribute__((always_inline)) {
    constexpr int i = decltype(ic)::value;
    if constexpr (i < KWIN) PACE(KWIN);
    else {
      PACE(VWIN);
      if (tnow - tb < (unsigned)((i - 1 + TOFF) * SLOT_NS / 10)) PACE(KWIN);
      __builtin_amdgcn_sched_barrier(0);
    }
    if constexpr (i == KWIN - 1) {
      if (isq && lane == 0) __hip_atomic_fetch_add((LAS unsigned*)(smem + L_FLAG + 12), 1u, __ATOMIC_RELAXED, __HIP_MEMORY_SCOPE_WORKGROUP);
    }
    if constexpr (i < NL) K[i] = __builtin_amdgcn_raw_buffer_load_b128(rs_k, voff, SOFF(i), AUX_NT);
    else V[i - NL] = __builtin_amdgcn_raw_buffer_load_b128(rs_v, voff, SOFF(i - NL), AUX_NT);
    __builtin_amdgcn_sched_barrier(0);
    tnow = (unsigned)__builtin_amdgcn_s_memrealtime();
    __builtin_amdgcn_sched_barrier(0);
    if constexpr (i == KWIN - 1) {
      if (!isq) __syncthreads();
    }
    if constexpr (i == 2 * NL - 1) {
      if (lane == 0) __hip_atomic_fetch_add((LAS unsigned*)(smem + L_FLAG + 20), 1u, __ATOMIC_RELAXED, __HIP_MEMORY_SCOPE_WORKGROUP);
      __builtin_amdgcn_sched_barrier(0);
    }
    if constexpr (i == QS) {
      while (flag[0] == 0u) __builtin_amdgcn_s_sleep(1);
      asm volatile("" ::: "memory");
      qA = *(LAS u32x4*)(smem + L_Q + hA * 256 + li * 16);
      qB = *(LAS u32x4*)(smem + L_Q + (hA ^ 1) * 256 + li * 16);
    }
    if constexpr (i >= QS && i < ES) {
#pragma unroll
      for (int u = 2 * (i - QS); u < 2 * (i - QS) + 2; ++u) {
        float xa = 0.f, xb = 0.f;
#pragma unroll
        for (int k = 0; k < 4; ++k) { xa = dot2(K[u][k], qA[k], xa); xb = dot2(K[u][k], qB[k], xb); }
        float t = xa + dpp<ROR8>(xb);
        t += dpp<XOR1>(t);
        t += dpp<XOR2>(t);
        t += dpp<HMIRROR>(t);
        y[u] = t;
        m = fmaxf(m, t);
        asm volatile("" : "+v"(y[u]), "+v"(m));
      }
    }
    if constexpr (i == ES) mc = xrow_max(m) * C2;
    if constexpr (i >= ES && i < PS) {
#pragma unroll
      for (int u = (NL / 4) * (i - ES); u < (NL / 4) * (i - ES + 1); ++u) { y[u] = __builtin_amdgcn_exp2f(fmaf(y[u], C2, -mc)); l += y[u]; }
      asm volatile("" : "+v"(l));
    }
    if constexpr (i == PS - 1) {
      l = xrow_sum(l);
      *(LAS f32x2*)(smem + L_ML + hA * (NW * 8) + sw * 8) = f32x2{mc, l};
      asm volatile("s_waitcnt lgkmcnt(0)" ::: "memory");
      if (lane == 0) __hip_atomic_fetch_add((LAS unsigned*)(smem + L_FLAG + 4), 1u, __ATOMIC_RELAXED, __HIP_MEMORY_SCOPE_WORKGROUP);
    }
    if constexpr (i >= PS) {
#pragma unroll
      for (int u = npv_after(i - 1); u < npv_after(i); ++u) PV(u);
      asm volatile("" : "+v"(acc));
    }
    if constexpr (i == FIN_AT) {
      __builtin_amdgcn_sched_barrier(0);
      while (flag[1] != (unsigned)NW) __builtin_amdgcn_s_sleep(1);
      asm volatile("" ::: "memory");
      f32x4 ml[NW / 2];
#pragma unroll
      for (int w = 0; w < NW / 2; ++w) ml[w] = *(LAS f32x4*)(smem + L_ML + hA * (NW * 8) + w * 16);
      float M = fmaxf(ml[0][0], ml[0][2]);
#pragma unroll
      for (int w = 1; w < NW / 2; ++w) M = fmaxf(M, fmaxf(ml[w][0], ml[w][2]));
      float L = 0.f;
#pragma unroll
      for (int w = 0; w < NW / 2; ++w) {
        L = fmaf(ml[w][1], __builtin_amdgcn_exp2f(ml[w][0] - M), L);
        L = fmaf(ml[w][3], __builtin_amdgcn_exp2f(ml[w][2] - M), L);
      }
      f = __builtin_amdgcn_exp2f(mc - M) / L;
      asm volatile("" : "+v"(f));
    }
  });
  __builtin_amdgcn_sched_barrier(0);
#pragma unroll
  for (int u = npv_after(2 * NL - 1); u < NL; ++u) PV(u);
  *(LAS f32x4*)(smem + L_RED + sw * 1024 + lane * 16) = acc * f;
  __syncthreads();
}

extern "C" void kernel_launch(void* const* d_in, const int* in_sizes, int n_in, void* d_out, int out_size,
                              void* d_ws, size_t ws_size, hipStream_t stream) {
  fused_decode_kernel<<<256, NT, 0, stream>>>((const unsigned char*)d_in[0], (const unsigned char*)d_in[1],
                                             (const unsigned char*)d_in[2], (const unsigned char*)d_in[3],
                                             (const unsigned char*)d_in[4], (unsigned char*)d_out,
                                             (unsigned char*)d_ws);
}
```

```cpp
#include <hip/hip_runtime.h>
#include <hip/hip_bf16.h>
#include <stdint.h>
#include <utility>

#define GAS __attribute__((address_space(1)))
#define LAS __attribute__((address_space(3)))
typedef unsigned u32x4 __attribute__((ext_vector_type(4)));
typedef unsigned u32x2 __attribute__((ext_vector_type(2)));
typedef float f32x4 __attribute__((ext_vector_type(4)));
typedef float f32x2 __attribute__((ext_vector_type(2)));
typedef short s16x4 __attribute__((ext_vector_type(4)));
typedef __bf16 bf16x8 __attribute__((ext_vector_type(8)));
typedef __bf16 bf16x2 __attribute__((ext_vector_type(2)));

namespace {

constexpr int NB = 16, DM = 576, HKV = 16, SK = 4096, DH = 128, WIN = 1024, NQ = 4096;
constexpr int NW = 8, NS = 4, NT = (NW + NS) * 64;
constexpr int NL = 256 / NW;

constexpr int KWIN = 3, VWIN = 5;
constexpr int SLOT_NS = 283, TOFF = 4;
constexpr int QS = 26;
constexpr int FIN_AT = 58;
constexpr int WOWIN = 3;
constexpr int AUX_NT = 2, AUX_SC1 = 16;
constexpr int RSRC_W3 = 0x00020000;
constexpr unsigned OOB = 0x7ffffff0u;

constexpr unsigned WS_EPOCH = 0;
constexpr unsigned WS_QG = 4096;
constexpr unsigned WS_OG = WS_QG + 256 * 1024;
constexpr unsigned WS_PG = WS_OG + 256 * 1024;
constexpr unsigned WS_BYTES = WS_PG + 36 * 16 * 2 * 1024;

constexpr int L_WQ = 0;
constexpr int L_X = L_WQ + 36864;
constexpr int L_WO = L_X + 18432;
constexpr int L_RED = L_WO + 32768;
constexpr int L_OA = L_RED + NW * 1024;
constexpr int L_Q = L_OA + 16 * 528;
constexpr int L_ML = L_Q + 512;
constexpr int L_FLAG = L_ML + NW * 16;
constexpr int L_BYTES = L_FLAG + 32;

constexpr unsigned SPIN_CAP = 1u << 18;

template <class F, int... I>
__device__ __forceinline__ void sfor_impl(F&& f, std::integer_sequence<int, I...>) { (f(std::integral_constant<int, I>{}), ...); }
template <int N, class F>
__device__ __forceinline__ void sfor(F&& f) { sfor_impl(f, std::make_integer_sequence<int, N>{}); }
constexpr int ES = QS + NL / 2, PS = ES + 4;
constexpr int npv_after(int slot) {
  int n = 0;
  for (int i = PS; i <= slot; ++i)
    for (int k = 0; k < 2; ++k)
      if (n <= i - NL - VWIN) ++n;
  return n;
}

__device__ __forceinline__ float dot2(unsigned k, unsigned q, float acc) {
  return __builtin_amdgcn_fdot2_f32_bf16(__builtin_bit_cast(bf16x2, k), __builtin_bit_cast(bf16x2, q), acc, false);
}
__device__ __forceinline__ unsigned pkbf(float lo, float hi) {
  f32x2 f = {lo, hi};
  return __builtin_bit_cast(unsigned, __builtin_convertvector(f, bf16x2));
}
template <int CTRL> __device__ __forceinline__ float dpp(float x) {
  return __builtin_bit_cast(float, __builtin_amdgcn_mov_dpp(__builtin_bit_cast(int, x), CTRL, 0xf, 0xf, true));
}
constexpr int XOR1 = 0xB1, XOR2 = 0x4E, HMIRROR = 0x141, ROR8 = 0x128;
__device__ __forceinline__ float xrow_max(float x) {
  auto s = __builtin_amdgcn_permlane16_swap(__float_as_uint(x), __float_as_uint(x), false, false);
  x = fmaxf(__uint_as_float(s[0]), __uint_as_float(s[1]));
  auto t = __builtin_amdgcn_permlane32_swap(__float_as_uint(x), __float_as_uint(x), false, false);
  return fmaxf(__uint_as_float(t[0]), __uint_as_float(t[1]));
}
__device__ __forceinline__ float xrow_sum(float x) {
  auto s = __builtin_amdgcn_permlane16_swap(__float_as_uint(x), __float_as_uint(x), false, false);
  x = __uint_as_float(s[0]) + __uint_as_float(s[1]);
  auto t = __builtin_amdgcn_permlane32_swap(__float_as_uint(x), __float_as_uint(x), false, false);
  return __uint_as_float(t[0]) + __uint_as_float(t[1]);
}
__device__ __forceinline__ f32x4 mfma16(u32x4 a, u32x4 b, f32x4 c) {
  return __builtin_amdgcn_mfma_f32_16x16x32_bf16(__builtin_bit_cast(bf16x8, a), __builtin_bit_cast(bf16x8, b), c, 0, 0, 0);
}
template <int NTP>
__device__ __forceinline__ void dma16(unsigned lds, unsigned voff, __amdgpu_buffer_rsrc_t rs, unsigned soff) {
  if constexpr (NTP == 1)
    asm volatile("s_mov_b32 m0, %0\n\ts_nop 0\n\tbuffer_load_dwordx4 %1, %2, %3 offen nt lds"
                 :: "s"(lds), "v"(voff), "s"(rs), "s"(soff) : "memory");
  else
    asm volatile("s_mov_b32 m0, %0\n\ts_nop 0\n\tbuffer_load_dwordx4 %1, %2, %3 offen lds"
                 :: "s"(lds), "v"(voff), "s"(rs), "s"(soff) : "memory");
}
__device__ __forceinline__ u32x2 trread(LAS unsigned char* p) {
  s16x4 v = __builtin_amdgcn_ds_read_tr16_b64_v4i16((LAS s16x4*)p);
  return __builtin_bit_cast(u32x2, v);
}
__device__ __forceinline__ unsigned wo_src(int lane) {
  const int row = ((lane >> 4) & 1) * 4 + ((lane >> 3) & 1) * 2 + (lane >> 5);
  return (unsigned)row * (DM * 2) + (unsigned)(lane & 7) * 16;
}
template <int STRIDE>
__device__ __forceinline__ void poll4(unsigned (&p)[8], __amdgpu_buffer_rsrc_t rs, unsigned off, unsigned tag) {
  unsigned cap = SPIN_CAP, t0, t1, t2;
#define PL(b) "buffer_load_dwordx4 v[" #b ":" #b "+3], %[off], %[rs], 0 offen sc1\n\t" \
              "buffer_load_dwordx4 v[" #b "+4:" #b "+7], %[off], %[rs], %[s1] offen sc1\n\t" \
              "buffer_load_dwordx4 v[" #b "+8:" #b "+11], %[off], %[rs], %[s2] offen sc1\n\t" \
              "buffer_load_dwordx4 v[" #b "+12:" #b "+15], %[off], %[rs], %[s3] offen sc1\n\t"
#define PC(b) "s_waitcnt vmcnt(4)\n\t" \
              "v_bitop3_b32 %[t0], v[" #b "+1], v[" #b "+3], %[tag] bitop3:0x7e\n\t" \
              "v_bitop3_b32 %[t1], v[" #b "+5], v[" #b "+7], %[tag] bitop3:0x7e\n\t" \
              "v_bitop3_b32 %[t2], v[" #b "+9], v[" #b "+11], %[tag] bitop3:0x7e\n\t" \
              "v_or3_b32 %[t0], %[t0], %[t1], %[t2]\n\t" \
              "v_bitop3_b32 %[t1], v[" #b "+13], v[" #b "+15], %[tag] bitop3:0x7e\n\t" \
              "v_or_b32 %[t0], %[t0], %[t1]\n\t" \
              "v_cmp_ne_u32 vcc, 0, %[t0]\n\t"
#define PM(b) "v_mov_b32 %[p0], v[" #b "]\n\tv_mov_b32 %[p1], v[" #b "+2]\n\tv_mov_b32 %[p2], v[" #b "+4]\n\tv_mov_b32 %[p3], v[" #b "+6]\n\t" \
              "v_mov_b32 %[p4], v[" #b "+8]\n\tv_mov_b32 %[p5], v[" #b "+10]\n\tv_mov_b32 %[p6], v[" #b "+12]\n\tv_mov_b32 %[p7], v[" #b "+14]\n\t"
  asm volatile(
      PL(136)
      "s_sleep %[dl]\n"
      "1:\n\t"
      PL(152)
      PC(136)
      "s_cbranch_vccz 2f\n\t"
      "s_sub_u32 %[cap], %[cap], 1\n\t"
      "s_cbranch_scc1 2f\n\t"
      PL(136)
      PC(152)
      "s_cbranch_vccnz 1b\n\t"
      PM(152)
      "s_branch 3f\n"
      "2:\n\t"
      PM(136)
      "3:\n"
      : [p0] "=&v"(p[0]), [p1] "=&v"(p[1]), [p2] "=&v"(p[2]), [p3] "=&v"(p[3]), [p4] "=&v"(p[4]), [p5] "=&v"(p[5]), [p6] "=&v"(p[6]), [p7] "=&v"(p[7]),
        [t0] "=&v"(t0), [t1] "=&v"(t1), [t2] "=&v"(t2), [cap] "+s"(cap)
      : [off] "v"(off), [rs] "s"(rs), [tag] "v"(tag), [s1] "s"(STRIDE), [s2] "s"(2 * STRIDE), [s3] "s"(3 * STRIDE), [dl] "i"(8)
      : "memory", "vcc", "scc",
        "v136", "v137", "v138", "v139", "v140", "v141", "v142", "v143", "v144", "v145", "v146", "v147", "v148", "v149", "v150", "v151",
        "v152", "v153", "v154", "v155", "v156", "v157", "v158", "v159", "v160", "v161", "v162", "v163", "v164", "v165", "v166", "v167");
#undef PL
#undef PC
#undef PM
}

}

extern "C" __global__ void __launch_bounds__(NT, (NW + NS) / 4)
fused_decode_kernel(const unsigned char* __restrict__ x, const unsigned char* __restrict__ kc,
                    const unsigned char* __restrict__ vc, const unsigned char* __restrict__ wq,
                    const unsigned char* __restrict__ wo, unsigned char* out, unsigned char* ws) {
  __shared__ __attribute__((aligned(16))) unsigned char smem_[L_BYTES];
  LAS unsigned char* smem = (LAS unsigned char*)smem_;
  const unsigned lds0 = (unsigned)(size_t)smem;
  const int tid = threadIdx.x, lane = tid & 63;
  const int wave = __builtin_amdgcn_readfirstlane(tid >> 6);
  const int bid = blockIdx.x, jj = bid >> 3, b = jj & 15, kvh = 2 * ((bid + (b >> 1)) & 7) + (jj >> 4);
  const int li = lane & 15, lg = lane >> 4;
  volatile LAS unsigned* flag = (volatile LAS unsigned*)(smem + L_FLAG);
#define PACE(n) do { __builtin_amdgcn_sched_barrier(0); asm volatile("s_waitcnt vmcnt(%0)" :: "i"((n) - 1) : "memory"); } while (0)

  if (wave >= NW) {
    const int t = wave - NW;
    const __amdgpu_buffer_rsrc_t rs_ws = __builtin_amdgcn_make_buffer_rsrc(ws, 0, WS_BYTES, RSRC_W3);
    if (t == 0) { flag[0] = 0u; flag[1] = 0u; flag[3] = 0u; flag[4] = 0u; flag[5] = 0u; }
    __builtin_amdgcn_s_setprio(3);
    if (t != 0) __syncthreads();
    if (t == 0) {
      if (b >= 8) {
        const __amdgpu_buffer_rsrc_t rs_x = __builtin_amdgcn_make_buffer_rsrc((void*)x, 0, NB * DM * 2, RSRC_W3);
        const unsigned vx = (unsigned)(lane >> 2) * (DM * 2) + (unsigned)(lane & 3) * 16;
#pragma unroll 1
        for (int s = 0; s < 18; ++s) dma16<0>(lds0 + L_X + s * 1024, vx, rs_x, (unsigned)s * 64);
      }
      unsigned epoch = __builtin_amdgcn_raw_buffer_load_b32(rs_ws, WS_EPOCH, 0, AUX_SC1);
      __builtin_amdgcn_sched_barrier(0);
      __syncthreads();
      asm volatile("s_waitcnt vmcnt(0)" : "+v"(epoch) :: "memory");
      const unsigned tg = epoch + 1u;
      flag[2] = tg;
      if (b >= 8) {
        while (flag[3] != (unsigned)NW) __builtin_amdgcn_s_sleep(1);
        asm volatile("" ::: "memory");
        f32x4 a0 = {0.f, 0.f, 0.f, 0.f}, a1 = {0.f, 0.f, 0.f, 0.f};
        LAS unsigned char* pw = smem + L_WQ + (8 * lg + ((lane >> 2) & 3)) * 64 + (lane & 3) * 8;
        LAS unsigned char* px = smem + L_X + li * 64 + lg * 16;
#pragma unroll 6
        for (int s = 0; s < 18; ++s) {
          const u32x2 w0 = trread(pw + s * 2048), w1 = trread(pw + s * 2048 + 256);
          const u32x2 w2 = trread(pw + s * 2048 + 32), w3 = trread(pw + s * 2048 + 256 + 32);
          const u32x4 xa = *(LAS u32x4*)(px + s * 1024);
          a0 = mfma16(xa, u32x4{w0[0], w0[1], w1[0], w1[1]}, a0);
          a1 = mfma16(xa, u32x4{w2[0], w2[1], w3[0], w3[1]}, a1);
        }
        LAS unsigned* tq = (LAS unsigned*)(smem + L_OA);
#pragma unroll
        for (int r = 0; r < 4; ++r) tq[(4 * lg + r) * 16 + li] = pkbf(a0[r], a1[r]);
        asm volatile("s_waitcnt lgkmcnt(0)" ::: "memory");
        const unsigned qo = WS_QG + (unsigned)(kvh * 16 + (lane >> 3)) * 1024 + (unsigned)(b - 8) * 128 + (unsigned)(lane & 7) * 16;
#pragma unroll
        for (int k = 0; k < 2; ++k) {
          const u32x2 v = *(LAS u32x2*)(tq + k * 128 + lane * 2);
          __builtin_amdgcn_raw_buffer_store_b128(u32x4{v[0], tg, v[1], tg}, rs_ws, qo, k * 8192, AUX_SC1);
        }
        __builtin_amdgcn_s_sleep(32);
      } else {
        __builtin_amdgcn_s_sleep(96);
      }
      const unsigned qi = WS_QG + (unsigned)(kvh * 16 + b) * 1024 + (unsigned)lane * 16;
      u32x4 qr;
      for (unsigned spins = 0;; ++spins) {
        asm volatile("" ::: "memory");
        qr = __builtin_amdgcn_raw_buffer_load_b128(rs_ws, qi, 0, AUX_SC1);
        if (__builtin_amdgcn_ballot_w64(((qr[1] ^ tg) | (qr[3] ^ tg)) != 0u) == 0ull || spins > SPIN_CAP) break;
        __builtin_amdgcn_s_sleep(4);
      }
      {
        LAS unsigned* ql = (LAS unsigned*)(smem + L_Q) + 16 * (lane >> 3) + (lane & 7);
        ql[0] = __builtin_amdgcn_perm(qr[2], qr[0], 0x05040100u);
        ql[8] = __builtin_amdgcn_perm(qr[2], qr[0], 0x07060302u);
      }
      asm volatile("s_waitcnt lgkmcnt(0)" ::: "memory");
      flag[0] = 1u;
    } else if (t == 1 && b == 0) {
      const __amdgpu_buffer_rsrc_t rs_wo = __builtin_amdgcn_make_buffer_rsrc((void*)wo, 0, NQ * DM * 2, RSRC_W3);
      const unsigned vwo = wo_src(lane);
#pragma unroll 1
      for (int i = 0; i < 32; ++i) {
        PACE(WOWIN);
        dma16<1>(lds0 + L_WQ + i * 1024, vwo ^ (unsigned)((i & 3) * 32), rs_wo, (unsigned)(kvh * 256 + 8 * i) * (DM * 2) + 8 * 128);
      }
      asm volatile("s_waitcnt vmcnt(0)" ::: "memory");
      flag[4] = 1u;
    }
    while (flag[0] == 0u) __builtin_amdgcn_s_sleep(16);
    asm volatile("" ::: "memory");
    const unsigned tag = flag[2];
    const __amdgpu_buffer_rsrc_t rs_out = __builtin_amdgcn_make_buffer_rsrc(out, 0, NB * DM * 2, RSRC_W3);
    u32x4 wf[8], wf9[8];
    LAS unsigned char* pwf = smem + ((lane >> 2) & 3) * 1024 + lg * 128 + ((t ^ (lane >> 2)) & 3) * 32 + (lane & 3) * 8;
#define LOADWF(dst, base) _Pragma("unroll") for (int s = 0; s < 8; ++s) { const int ro = ((s & 1) * 128 + 32 * (s >> 1)) * 128; \
        const u32x2 w0 = trread(pwf + (base) + ro), w1 = trread(pwf + (base) + ro + 512); dst[s] = u32x4{w0[0], w0[1], w1[0], w1[1]}; }
    if (b == 0) {
      while (flag[4] == 0u) __builtin_amdgcn_s_sleep(16);
      asm volatile("" ::: "memory");
      LOADWF(wf9, L_WQ)
    }
    const int rho = (b - 8) * 16 + kvh, T = rho >> 1, hf = rho & 1;
    unsigned o_pub = WS_OG + (unsigned)(kvh * 16 + b) * 1024 + (unsigned)lane * 16;
    unsigned o_get = b < 8 ? WS_OG + (unsigned)(kvh * 16 + 4 * t) * 1024 + (unsigned)lane * 16
                           : WS_PG + (unsigned)((T * 16 + lg) * 2 + hf) * 1024 + (unsigned)(16 * t + li) * 16;
    unsigned o_put = b < 8 ? WS_PG + (unsigned)(((4 * b + t) * 16 + kvh) * 2) * 1024 + (unsigned)lane * 16
                           : (lg == 0 ? (unsigned)(li * DM + 16 * T + 4 * t + 2 * hf) * 2 : OOB);
    asm volatile("" : "+v"(o_pub), "+v"(o_get), "+v"(o_put));
    if (b < 8 && t > 0) {
      const __amdgpu_buffer_rsrc_t rs_wo = __builtin_amdgcn_make_buffer_rsrc((void*)wo, 0, NQ * DM * 2, RSRC_W3);
      const unsigned vwo = wo_src(lane);
      while (flag[5] != (unsigned)NW) __builtin_amdgcn_s_sleep(2);
#pragma unroll 1
      for (int i = t - 1; i < 32; i += 3)
        dma16<1>(lds0 + L_WO + i * 1024, vwo ^ (unsigned)((i & 3) * 32), rs_wo, (unsigned)(kvh * 256 + 8 * i) * (DM * 2) + (unsigned)b * 128);
    }
    __syncthreads();
    if (t == 0) {
      f32x4 pr[NW];
#pragma unroll
      for (int w = 0; w < NW; ++w) pr[w] = *(LAS f32x4*)(smem + L_RED + w * 1024 + lane * 16);
      __builtin_amdgcn_sched_barrier(0);
#pragma unroll
      for (int s = 1; s < NW; s *= 2)
#pragma unroll
        for (int w = 0; w < NW; w += 2 * s) pr[w] += pr[w + s];
      const f32x4 o = pr[0];
      __builtin_amdgcn_raw_buffer_store_b128(u32x4{pkbf(o[0], o[1]), tag, pkbf(o[2], o[3]), tag}, rs_ws, o_pub, 0, AUX_SC1);
    }
    unsigned p[8];
    if (b < 8) {
      if (t > 0) {
        asm volatile("s_waitcnt vmcnt(0)" ::: "memory");
        if (lane == 0) __hip_atomic_fetch_add((LAS unsigned*)(smem + L_FLAG + 12), 1u, __ATOMIC_RELAXED, __HIP_MEMORY_SCOPE_WORKGROUP);
      }
      while (flag[3] != 3u) __builtin_amdgcn_s_sleep(1);
      asm volatile("" ::: "memory");
      LOADWF(wf, L_WO)
      __builtin_amdgcn_sched_barrier(0);
      poll4<1024>(p, rs_ws, o_get, tag);
#pragma unroll
      for (int i = 0; i < 4; ++i) *(LAS u32x2*)(smem + L_OA + (4 * t + i) * 528 + lane * 8) = u32x2{p[2 * i], p[2 * i + 1]};
      __syncthreads();
      u32x4 ob[8];
#pragma unroll
      for (int s = 0; s < 8; ++s) ob[s] = *(LAS u32x4*)(smem + L_OA + li * 528 + s * 64 + lg * 16);
      __builtin_amdgcn_sched_barrier(0);
      f32x4 d = {0.f, 0.f, 0.f, 0.f};
#pragma unroll
      for (int s = 0; s < 8; ++s) d = mfma16(wf[s], ob[s], d);
      __builtin_amdgcn_raw_buffer_store_b128(u32x4{__float_as_uint(d[0]), tag, __float_as_uint(d[1]), tag}, rs_ws, o_put, 0, AUX_SC1);
      __builtin_amdgcn_raw_buffer_store_b128(u32x4{__float_as_uint(d[2]), tag, __float_as_uint(d[3]), tag}, rs_ws, o_put, 1024, AUX_SC1);
      if (b == 0) {
        f32x4 e = {0.f, 0.f, 0.f, 0.f};
#pragma unroll
        for (int s = 0; s < 8; ++s) e = mfma16(wf9[s], ob[s], e);
        __builtin_amdgcn_raw_buffer_store_b128(u32x4{__float_as_uint(e[0]), tag, __float_as_uint(e[1]), tag}, rs_ws, o_put, 32 * 32768, AUX_SC1);
        __builtin_amdgcn_raw_buffer_store_b128(u32x4{__float_as_uint(e[2]), tag, __float_as_uint(e[3]), tag}, rs_ws, o_put, 32 * 32768 + 1024, AUX_SC1);
      }
    } else if (rho < 72) {
      poll4<4 * 2048>(p, rs_ws, o_get, tag);
      float s0 = (__uint_as_float(p[0]) + __uint_as_float(p[2])) + (__uint_as_float(p[4]) + __uint_as_float(p[6]));
      float s1 = (__uint_as_float(p[1]) + __uint_as_float(p[3])) + (__uint_as_float(p[5]) + __uint_as_float(p[7]));
      s0 = xrow_sum(s0); s1 = xrow_sum(s1);
      __builtin_amdgcn_raw_buffer_store_b32(pkbf(s0, s1), rs_out, o_put, 0, 0);
      if (rho == 0 && t == 0) __builtin_amdgcn_raw_buffer_store_b32(tag, rs_ws, WS_EPOCH, 0, AUX_SC1);
    }
    asm volatile("s_waitcnt vmcnt(0)" ::: "memory");
    return;
  }

  const int sw = wave;
  const unsigned tb = (unsigned)__builtin_amdgcn_s_memrealtime();
  const bool isq = b >= 8;
  if (isq) {
    const __amdgpu_buffer_rsrc_t rs_w = __builtin_amdgcn_make_buffer_rsrc((void*)wq, 0, DM * NQ * 2, RSRC_W3);
    const unsigned vw = (unsigned)(lane >> 2) * (NQ * 2) + (unsigned)(lane & 3) * 16 + (unsigned)(kvh * 256 + 32 * (b - 8)) * 2;
    int cnt = 0;
#pragma unroll 1
    for (int i = sw; i < 36; i += NW) {
      if (cnt >= KWIN) PACE(KWIN);
      dma16<1>(lds0 + L_WQ + i * 1024, vw, rs_w, (unsigned)i * (16 * NQ * 2));
      if (++cnt == KWIN) __syncthreads();
    }
    static_assert(KWIN <= 4, "every streaming wave must reach the barrier inside the weight loop");
  }
  const size_t kvoff = ((size_t)(b * HKV + kvh) * SK + (SK - WIN)) * (DH * 2);
  const __amdgpu_buffer_rsrc_t rs_k = __builtin_amdgcn_make_buffer_rsrc((void*)(kc + kvoff), 0, WIN * DH * 2, RSRC_W3);
  const __amdgpu_buffer_rsrc_t rs_v = __builtin_amdgcn_make_buffer_rsrc((void*)(vc + kvoff), 0, WIN * DH * 2, RSRC_W3);
  const unsigned voff = (unsigned)lane * 16 + (unsigned)sw * (NL * 1024);
  const unsigned ph = (unsigned)sw * 4096;
#define SOFF(u) (((u) * 1024 + ph) & (NL * 1024 - 1))
  u32x4 K[NL], V[NL], qA, qB;
  float y[NL], m = -INFINITY, mc = 0.f, l = 0.f, f = 0.f;
  const int hA = li >> 3;
  constexpr float C2 = 0.088388347648318440f * 1.4426950408889634f;
  const unsigned hm = (lane & 1) ? 0xffff0000u : 0x0000ffffu;
  const int rsel = (lane & 7) >> 1;
  const unsigned mk0 = rsel == 0 ? hm : 0u, mk1 = rsel == 1 ? hm : 0u, mk2 = rsel == 2 ? hm : 0u, mk3 = rsel == 3 ? hm : 0u;
  f32x4 acc = {0.f, 0.f, 0.f, 0.f};
  unsigned tnow = tb;
#define PV(u) do { const unsigned pp = pkbf(y[u], y[u]); acc = mfma16(V[u], u32x4{pp & mk0, pp & mk1, pp & mk2, pp & mk3}, acc); } while (0)
  sfor<2 * NL>([&](auto ic) __attribute__((always_inline)) {
    constexpr int i = decltype(ic)::value;
    if constexpr (i < KWIN) PACE(KWIN);
    else {
      PACE(VWIN);
      if (tnow - tb < (unsigned)((i - 1 + TOFF) * SLOT_NS / 10)) PACE(KWIN);
      __builtin_amdgcn_sched_barrier(0);
    }
    if constexpr (i == KWIN - 1) {
      if (isq && lane == 0) __hip_atomic_fetch_add((LAS unsigned*)(smem + L_FLAG + 12), 1u, __ATOMIC_RELAXED, __HIP_MEMORY_SCOPE_WORKGROUP);
    }
    if constexpr (i < NL) K[i] = __builtin_amdgcn_raw_buffer_load_b128(rs_k, voff, SOFF(i), AUX_NT);
    else V[i - NL] = __builtin_amdgcn_raw_buffer_load_b128(rs_v, voff, SOFF(i - NL), AUX_NT);
    __builtin_amdgcn_sched_barrier(0);
    tnow = (unsigned)__builtin_amdgcn_s_memrealtime();
    __builtin_amdgcn_sched_barrier(0);
    if constexpr (i == KWIN - 1) {
      if (!isq) __syncthreads();
    }
    if constexpr (i == 2 * NL - 1) {
      if (lane == 0) __hip_atomic_fetch_add((LAS unsigned*)(smem + L_FLAG + 20), 1u, __ATOMIC_RELAXED, __HIP_MEMORY_SCOPE_WORKGROUP);
      __builtin_amdgcn_sched_barrier(0);
    }
    if constexpr (i == QS) {
      while (flag[0] == 0u) __builtin_amdgcn_s_sleep(1);
      asm volatile("" ::: "memory");
      qA = *(LAS u32x4*)(smem + L_Q + hA * 256 + li * 16);
      qB = *(LAS u32x4*)(smem + L_Q + (hA ^ 1) * 256 + li * 16);
    }
    if constexpr (i >= QS && i < ES) {
#pragma unroll
      for (int u = 2 * (i - QS); u < 2 * (i - QS) + 2; ++u) {
        float xa = 0.f, xb = 0.f;
#pragma unroll
        for (int k = 0; k < 4; ++k) { xa = dot2(K[u][k], qA[k], xa); xb = dot2(K[u][k], qB[k], xb); }
        float t = xa + dpp<ROR8>(xb);
        t += dpp<XOR1>(t);
        t += dpp<XOR2>(t);
        t += dpp<HMIRROR>(t);
        y[u] = t;
        m = fmaxf(m, t);
        asm volatile("" : "+v"(y[u]), "+v"(m));
      }
    }
    if constexpr (i == ES) mc = xrow_max(m) * C2;
    if constexpr (i >= ES && i < PS) {
#pragma unroll
      for (int u = (NL / 4) * (i - ES); u < (NL / 4) * (i - ES + 1); ++u) { y[u] = __builtin_amdgcn_exp2f(fmaf(y[u], C2, -mc)); l += y[u]; }
      asm volatile("" : "+v"(l));
    }
    if constexpr (i == PS - 1) {
      l = xrow_sum(l);
      *(LAS f32x2*)(smem + L_ML + hA * (NW * 8) + sw * 8) = f32x2{mc, l};
      asm volatile("s_waitcnt lgkmcnt(0)" ::: "memory");
      if (lane == 0) __hip_atomic_fetch_add((LAS unsigned*)(smem + L_FLAG + 4), 1u, __ATOMIC_RELAXED, __HIP_MEMORY_SCOPE_WORKGROUP);
    }
    if constexpr (i >= PS) {
#pragma unroll
      for (int u = npv_after(i - 1); u < npv_after(i); ++u) PV(u);
      asm volatile("" : "+v"(acc));
    }
    if constexpr (i == FIN_AT) {
      __builtin_amdgcn_sched_barrier(0);
      while (flag[1] != (unsigned)NW) __builtin_amdgcn_s_sleep(1);
      asm volatile("" ::: "memory");
      f32x4 ml[NW / 2];
#pragma unroll
      for (int w = 0; w < NW / 2; ++w) ml[w] = *(LAS f32x4*)(smem + L_ML + hA * (NW * 8) + w * 16);
      float M = fmaxf(ml[0][0], ml[0][2]);
#pragma unroll
      for (int w = 1; w < NW / 2; ++w) M = fmaxf(M, fmaxf(ml[w][0], ml[w][2]));
      float L = 0.f;
#pragma unroll
      for (int w = 0; w < NW / 2; ++w) {
        L = fmaf(ml[w][1], __builtin_amdgcn_exp2f(ml[w][0] - M), L);
        L = fmaf(ml[w][3], __builtin_amdgcn_exp2f(ml[w][2] - M), L);
      }
      f = __builtin_amdgcn_exp2f(mc - M) / L;
      asm volatile("" : "+v"(f));
    }
  });
  __builtin_amdgcn_sched_barrier(0);
#pragma unroll
  for (int u = npv_after(2 * NL - 1); u < NL; ++u) PV(u);
  *(LAS f32x4*)(smem + L_RED + sw * 1024 + lane * 16) = acc * f;
  __syncthreads();
}

extern "C" void kernel_launch(void* const* d_in, const int* in_sizes, int n_in, void* d_out, int out_size,
                              void* d_ws, size_t ws_size, hipStream_t stream) {
  fused_decode_kernel<<<256, NT, 0, stream>>>((const unsigned char*)d_in[0], (const unsigned char*)d_in[1],
                                             (const unsigned char*)d_in[2], (const unsigned char*)d_in[3],
                                             (const unsigned char*)d_in[4], (unsigned char*)d_out,
                                             (unsigned char*)d_ws + ((ws_size - WS_BYTES - (8u << 20)) & ~(size_t)0x1fffff));
}
```

```cpp
#include <hip/hip_runtime.h>
#include <hip/hip_bf16.h>
#include <stdint.h>
#include <utility>

#define LAS __attribute__((address_space(3)))
typedef unsigned u32x4 __attribute__((ext_vector_type(4)));
typedef unsigned u32x2 __attribute__((ext_vector_type(2)));
typedef float f32x4 __attribute__((ext_vector_type(4)));
typedef float f32x2 __attribute__((ext_vector_type(2)));
typedef short s16x4 __attribute__((ext_vector_type(4)));
typedef __bf16 bf16x8 __attribute__((ext_vector_type(8)));
typedef __bf16 bf16x2 __attribute__((ext_vector_type(2)));

namespace {

constexpr int NB = 16, DM = 576, HKV = 16, SK = 4096, DH = 128, WIN = 1024, NQ = 4096;
constexpr int NW = 8, NS = 4, NT = (NW + NS) * 64;
constexpr int NL = 256 / NW;

constexpr int KWIN = 3, VWIN = 5;
constexpr int SLOT_NS = 283, TOFF = 4;
constexpr int QS = 26;
constexpr int FIN_AT = 58;
constexpr int AUX_NT = 2, AUX_SC1 = 16;
constexpr int RSRC_W3 = 0x00020000;
constexpr unsigned OOB = 0x7ffffff0u;

constexpr unsigned WS_EPOCH = 0;
constexpr unsigned WS_QG = 4096;
constexpr unsigned WS_OG = WS_QG + 256 * 1024;
constexpr unsigned WS_PG = WS_OG + 256 * 1024;
constexpr unsigned WS_BYTES = WS_PG + 36 * 16 * 2 * 1024;

constexpr int L_WQ = 0;
constexpr int L_X = L_WQ + 36864;
constexpr int L_WO = L_X + 18432;
constexpr int L_RED = L_WO + 32768;
constexpr int L_OA = L_RED + NW * 1024;
constexpr int L_Q = L_OA + 16 * 528;
constexpr int L_ML = L_Q + 512;
constexpr int L_FLAG = L_ML + NW * 16;
constexpr int L_BYTES = L_FLAG + 32;

constexpr unsigned SPIN_CAP = 1u << 18;

template <class F, int... I>
__device__ __forceinline__ void sfor_impl(F&& f, std::integer_sequence<int, I...>) { (f(std::integral_constant<int, I>{}), ...); }
template <int N, class F>
__device__ __forceinline__ void sfor(F&& f) { sfor_impl(f, std::make_integer_sequence<int, N>{}); }
constexpr int ES = QS + NL / 2, PS = ES + 4;
constexpr int npv_after(int slot) {
  int n = 0;
  for (int i = PS; i <= slot; ++i)
    for (int k = 0; k < 2; ++k)
      if (n <= i - NL - VWIN) ++n;
  return n;
}

__device__ __forceinline__ float dot2(unsigned k, unsigned q, float acc) {
  return __builtin_amdgcn_fdot2_f32_bf16(__builtin_bit_cast(bf16x2, k), __builtin_bit_cast(bf16x2, q), acc, false);
}
__device__ __forceinline__ unsigned pkbf(float lo, float hi) {
  f32x2 f = {lo, hi};
  return __builtin_bit_cast(unsigned, __builtin_convertvector(f, bf16x2));
}
template <int CTRL> __device__ __forceinline__ float dpp(float x) {
  return __builtin_bit_cast(float, __builtin_amdgcn_mov_dpp(__builtin_bit_cast(int, x), CTRL, 0xf, 0xf, true));
}
constexpr int XOR1 = 0xB1, XOR2 = 0x4E, HMIRROR = 0x141, ROR8 = 0x128;
__device__ __forceinline__ float xrow_max(float x) {
  auto s = __builtin_amdgcn_permlane16_swap(__float_as_uint(x), __float_as_uint(x), false, false);
  x = fmaxf(__uint_as_float(s[0]), __uint_as_float(s[1]));
  auto t = __builtin_amdgcn_permlane32_swap(__float_as_uint(x), __float_as_uint(x), false, false);
  return fmaxf(__uint_as_float(t[0]), __uint_as_float(t[1]));
}
__device__ __forceinline__ float xrow_sum(float x) {
  auto s = __builtin_amdgcn_permlane16_swap(__float_as_uint(x), __float_as_uint(x), false, false);
  x = __uint_as_float(s[0]) + __uint_as_float(s[1]);
  auto t = __builtin_amdgcn_permlane32_swap(__float_as_uint(x), __float_as_uint(x), false, false);
  return __uint_as_float(t[0]) + __uint_as_float(t[1]);
}
__device__ __forceinline__ f32x4 mfma16(u32x4 a, u32x4 b, f32x4 c) {
  return __builtin_amdgcn_mfma_f32_16x16x32_bf16(__builtin_bit_cast(bf16x8, a), __builtin_bit_cast(bf16x8, b), c, 0, 0, 0);
}
template <int NTP>
__device__ __forceinline__ void dma16(unsigned lds, unsigned voff, __amdgpu_buffer_rsrc_t rs, unsigned soff) {
  if constexpr (NTP == 1)
    asm volatile("s_mov_b32 m0, %0\n\ts_nop 0\n\tbuffer_load_dwordx4 %1, %2, %3 offen nt lds"
                 :: "s"(lds), "v"(voff), "s"(rs), "s"(soff) : "memory");
  else
    asm volatile("s_mov_b32 m0, %0\n\ts_nop 0\n\tbuffer_load_dwordx4 %1, %2, %3 offen lds"
                 :: "s"(lds), "v"(voff), "s"(rs), "s"(soff) : "memory");
}
__device__ __forceinline__ u32x2 trread(LAS unsigned char* p) {
  s16x4 v = __builtin_amdgcn_ds_read_tr16_b64_v4i16((LAS s16x4*)p);
  return __builtin_bit_cast(u32x2, v);
}
__device__ __forceinline__ unsigned wo_src(int lane) {
  const int row = ((lane >> 4) & 1) * 4 + ((lane >> 3) & 1) * 2 + (lane >> 5);
  return (unsigned)row * (DM * 2) + (unsigned)(lane & 7) * 16;
}
template <int STRIDE>
__device__ __forceinline__ void poll4(unsigned (&p)[8], __amdgpu_buffer_rsrc_t rs, unsigned off, unsigned tag) {
  unsigned cap = SPIN_CAP, t0, t1, t2;
#define PL(b) "buffer_load_dwordx4 v[" #b ":" #b "+3], %[off], %[rs], 0 offen sc1\n\t" \
              "buffer_load_dwordx4 v[" #b "+4:" #b "+7], %[off], %[rs], %[s1] offen sc1\n\t" \
              "buffer_load_dwordx4 v[" #b "+8:" #b "+11], %[off], %[rs], %[s2] offen sc1\n\t" \
              "buffer_load_dwordx4 v[" #b "+12:" #b "+15], %[off], %[rs], %[s3] offen sc1\n\t"
#define PC(b) "s_waitcnt vmcnt(4)\n\t" \
              "v_bitop3_b32 %[t0], v[" #b "+1], v[" #b "+3], %[tag] bitop3:0x7e\n\t" \
              "v_bitop3_b32 %[t1], v[" #b "+5], v[" #b "+7], %[tag] bitop3:0x7e\n\t" \
              "v_bitop3_b32 %[t2], v[" #b "+9], v[" #b "+11], %[tag] bitop3:0x7e\n\t" \
              "v_or3_b32 %[t0], %[t0], %[t1], %[t2]\n\t" \
              "v_bitop3_b32 %[t1], v[" #b "+13], v[" #b "+15], %[tag] bitop3:0x7e\n\t" \
              "v_or_b32 %[t0], %[t0], %[t1]\n\t" \
              "v_cmp_ne_u32 vcc, 0, %[t0]\n\t"
#define PM(b) "v_mov_b32 %[p0], v[" #b "]\n\tv_mov_b32 %[p1], v[" #b "+2]\n\tv_mov_b32 %[p2], v[" #b "+4]\n\tv_mov_b32 %[p3], v[" #b "+6]\n\t" \
              "v_mov_b32 %[p4], v[" #b "+8]\n\tv_mov_b32 %[p5], v[" #b "+10]\n\tv_mov_b32 %[p6], v[" #b "+12]\n\tv_mov_b32 %[p7], v[" #b "+14]\n\t"
  asm volatile(
      PL(136)
      "s_sleep %[dl]\n"
      "1:\n\t"
      PL(152)
      PC(136)
      "s_cbranch_vccz 2f\n\t"
      "s_sub_u32 %[cap], %[cap], 1\n\t"
      "s_cbranch_scc1 2f\n\t"
      PL(136)
      PC(152)
      "s_cbranch_vccnz 1b\n\t"
      PM(152)
      "s_branch 3f\n"
      "2:\n\t"
      PM(136)
      "3:\n"
      : [p0] "=&v"(p[0]), [p1] "=&v"(p[1]), [p2] "=&v"(p[2]), [p3] "=&v"(p[3]), [p4] "=&v"(p[4]), [p5] "=&v"(p[5]), [p6] "=&v"(p[6]), [p7] "=&v"(p[7]),
        [t0] "=&v"(t0), [t1] "=&v"(t1), [t2] "=&v"(t2), [cap] "+s"(cap)
      : [off] "v"(off), [rs] "s"(rs), [tag] "v"(tag), [s1] "s"(STRIDE), [s2] "s"(2 * STRIDE), [s3] "s"(3 * STRIDE), [dl] "i"(8)
      : "memory", "vcc", "scc",
        "v136", "v137", "v138", "v139", "v140", "v141", "v142", "v143", "v144", "v145", "v146", "v147", "v148", "v149", "v150", "v151",
        "v152", "v153", "v154", "v155", "v156", "v157", "v158", "v159", "v160", "v161", "v162", "v163", "v164", "v165", "v166", "v167");
#undef PL
#undef PC
#undef PM
}

}

extern "C" __global__ void __launch_bounds__(NT, (NW + NS) / 4)
fused_decode_kernel(const unsigned char* __restrict__ x, const unsigned char* __restrict__ kc,
                    const unsigned char* __restrict__ vc, const unsigned char* __restrict__ wq,
                    const unsigned char* __restrict__ wo, unsigned char* out, unsigned char* ws) {
  __shared__ __attribute__((aligned(16))) unsigned char smem_[L_BYTES];
  LAS unsigned char* smem = (LAS unsigned char*)smem_;
  const unsigned lds0 = (unsigned)(size_t)smem;
  const int tid = threadIdx.x, lane = tid & 63;
  const int wave = __builtin_amdgcn_readfirstlane(tid >> 6);
  const int bid = blockIdx.x, jj = bid >> 3, b = jj & 15, kvh = 2 * ((bid + (b >> 1)) & 7) + (jj >> 4);
  const int li = lane & 15, lg = lane >> 4;
  volatile LAS unsigned* flag = (volatile LAS unsigned*)(smem + L_FLAG);
#define PACE(n) do { __builtin_amdgcn_sched_barrier(0); asm volatile("s_waitcnt vmcnt(%0)" :: "i"((n) - 1) : "memory"); } while (0)

  if (wave >= NW) {
    const int t = wave - NW;
    const __amdgpu_buffer_rsrc_t rs_ws = __builtin_amdgcn_make_buffer_rsrc(ws, 0, WS_BYTES, RSRC_W3);
    if (t == 0) { flag[0] = 0u; flag[1] = 0u; flag[3] = 0u; flag[4] = 0u; flag[5] = 0u; }
    __builtin_amdgcn_s_setprio(3);
    if (t != 0) __syncthreads();
    if (t == 0) {
      if (b >= 8) {
        const __amdgpu_buffer_rsrc_t rs_x = __builtin_amdgcn_make_buffer_rsrc((void*)x, 0, NB * DM * 2, RSRC_W3);
        const unsigned vx = (unsigned)(lane >> 2) * (DM * 2) + (unsigned)(lane & 3) * 16;
#pragma unroll 1
        for (int s = 0; s < 18; ++s) dma16<0>(lds0 + L_X + s * 1024, vx, rs_x, (unsigned)s * 64);
      }
      unsigned epoch = __builtin_amdgcn_raw_buffer_load_b32(rs_ws, WS_EPOCH, 0, AUX_SC1);
      __builtin_amdgcn_sched_barrier(0);
      __syncthreads();
      asm volatile("s_waitcnt vmcnt(0)" : "+v"(epoch) :: "memory");
      const unsigned tg = epoch + 1u;
      flag[2] = tg;
      if (b >= 8) {
        while (flag[3] != (unsigned)NW) __builtin_amdgcn_s_sleep(1);
        asm volatile("" ::: "memory");
        f32x4 a0 = {0.f, 0.f, 0.f, 0.f}, a1 = {0.f, 0.f, 0.f, 0.f};
        LAS unsigned char* pw = smem + L_WQ + (8 * lg + ((lane >> 2) & 3)) * 64 + (lane & 3) * 8;
        LAS unsigned char* px = smem + L_X + li * 64 + lg * 16;
#pragma unroll 6
        for (int s = 0; s < 18; ++s) {
          const u32x2 w0 = trread(pw + s * 2048), w1 = trread(pw + s * 2048 + 256);
          const u32x2 w2 = trread(pw + s * 2048 + 32), w3 = trread(pw + s * 2048 + 256 + 32);
          const u32x4 xa = *(LAS u32x4*)(px + s * 1024);
          a0 = mfma16(xa, u32x4{w0[0], w0[1], w1[0], w1[1]}, a0);
          a1 = mfma16(xa, u32x4{w2[0], w2[1], w3[0], w3[1]}, a1);
        }
        LAS unsigned* tq = (LAS unsigned*)(smem + L_OA);
#pragma unroll
        for (int r = 0; r < 4; ++r) tq[(4 * lg + r) * 16 + li] = pkbf(a0[r], a1[r]);
        asm volatile("s_waitcnt lgkmcnt(0)" ::: "memory");
        const unsigned qo = WS_QG + (unsigned)(kvh * 16 + (lane >> 3)) * 1024 + (unsigned)(b - 8) * 128 + (unsigned)(lane & 7) * 16;
#pragma unroll
        for (int k = 0; k < 2; ++k) {
          const u32x2 v = *(LAS u32x2*)(tq + k * 128 + lane * 2);
          __builtin_amdgcn_raw_buffer_store_b128(u32x4{v[0], tg, v[1], tg}, rs_ws, qo, k * 8192, AUX_SC1);
        }
        __builtin_amdgcn_s_sleep(32);
      } else {
        __builtin_amdgcn_s_sleep(96);
      }
      const unsigned qi = WS_QG + (unsigned)(kvh * 16 + b) * 1024 + (unsigned)lane * 16;
      u32x4 qr;
      for (unsigned spins = 0;; ++spins) {
        asm volatile("" ::: "memory");
        qr = __builtin_amdgcn_raw_buffer_load_b128(rs_ws, qi, 0, AUX_SC1);
        if (__builtin_amdgcn_ballot_w64(((qr[1] ^ tg) | (qr[3] ^ tg)) != 0u) == 0ull || spins > SPIN_CAP) break;
        __builtin_amdgcn_s_sleep(4);
      }
      {
        LAS unsigned* ql = (LAS unsigned*)(smem + L_Q) + 16 * (lane >> 3) + (lane & 7);
        ql[0] = __builtin_amdgcn_perm(qr[2], qr[0], 0x05040100u);
        ql[8] = __builtin_amdgcn_perm(qr[2], qr[0], 0x07060302u);
      }
      asm volatile("s_waitcnt lgkmcnt(0)" ::: "memory");
      flag[0] = 1u;
    }
    while (flag[0] == 0u) __builtin_amdgcn_s_sleep(16);
    asm volatile("" ::: "memory");
    const unsigned tag = flag[2];
    const __amdgpu_buffer_rsrc_t rs_out = __builtin_amdgcn_make_buffer_rsrc(out, 0, NB * DM * 2, RSRC_W3);
    u32x4 wf[8];
    LAS unsigned char* pwf = smem + ((lane >> 2) & 3) * 1024 + lg * 128 + ((t ^ (lane >> 2)) & 3) * 32 + (lane & 3) * 8;
#define LOADWF(dst, base) _Pragma("unroll") for (int s = 0; s < 8; ++s) { const int ro = ((s & 1) * 128 + 32 * (s >> 1)) * 128; \
        const u32x2 w0 = trread(pwf + (base) + ro), w1 = trread(pwf + (base) + ro + 512); dst[s] = u32x4{w0[0], w0[1], w1[0], w1[1]}; }
    const bool st1 = b < 8 || b == 13;
    const int cs = b < 8 ? b : 8;
    const int rho = (b - 8) * 16 + kvh, T = rho >> 1, hf = rho & 1;
    unsigned o_pub = WS_OG + (unsigned)(kvh * 16 + b) * 1024 + (unsigned)lane * 16;
    unsigned o_get = st1 ? WS_OG + (unsigned)(kvh * 16 + 4 * t) * 1024 + (unsigned)lane * 16
                         : WS_PG + (unsigned)((T * 16 + lg) * 2 + hf) * 1024 + (unsigned)(16 * t + li) * 16;
    unsigned o_put = st1 ? WS_PG + (unsigned)(((4 * cs + t) * 16 + kvh) * 2) * 1024 + (unsigned)lane * 16
                         : (lg == 0 ? (unsigned)(li * DM + 16 * T + 4 * t + 2 * hf) * 2 : OOB);
    asm volatile("" : "+v"(o_pub), "+v"(o_get), "+v"(o_put));
    if (st1 && t > 0) {
      const __amdgpu_buffer_rsrc_t rs_wo = __builtin_amdgcn_make_buffer_rsrc((void*)wo, 0, NQ * DM * 2, RSRC_W3);
      const unsigned vwo = wo_src(lane);
      while (flag[5] != (unsigned)NW) __builtin_amdgcn_s_sleep(2);
#pragma unroll 1
      for (int i = t - 1; i < 32; i += 3)
        dma16<1>(lds0 + L_WO + i * 1024, vwo ^ (unsigned)((i & 3) * 32), rs_wo, (unsigned)(kvh * 256 + 8 * i) * (DM * 2) + (unsigned)cs * 128);
    }
    __syncthreads();
    if (t == 0) {
      f32x4 pr[NW];
#pragma unroll
      for (int w = 0; w < NW; ++w) pr[w] = *(LAS f32x4*)(smem + L_RED + w * 1024 + lane * 16);
      __builtin_amdgcn_sched_barrier(0);
#pragma unroll
      for (int s = 1; s < NW; s *= 2)
#pragma unroll
        for (int w = 0; w < NW; w += 2 * s) pr[w] += pr[w + s];
      const f32x4 o = pr[0];
      __builtin_amdgcn_raw_buffer_store_b128(u32x4{pkbf(o[0], o[1]), tag, pkbf(o[2], o[3]), tag}, rs_ws, o_pub, 0, AUX_SC1);
    }
    unsigned p[8];
    if (st1) {
      if (t > 0) {
        asm volatile("s_waitcnt vmcnt(0)" ::: "memory");
        if (lane == 0) __hip_atomic_fetch_add((LAS unsigned*)(smem + L_FLAG + 16), 1u, __ATOMIC_RELAXED, __HIP_MEMORY_SCOPE_WORKGROUP);
      }
      while (flag[4] != 3u) __builtin_amdgcn_s_sleep(1);
      asm volatile("" ::: "memory");
      LOADWF(wf, L_WO)
      __builtin_amdgcn_sched_barrier(0);
      poll4<1024>(p, rs_ws, o_get, tag);
#pragma unroll
      for (int i = 0; i < 4; ++i) *(LAS u32x2*)(smem + L_OA + (4 * t + i) * 528 + lane * 8) = u32x2{p[2 * i], p[2 * i + 1]};
      __syncthreads();
      u32x4 ob[8];
#pragma unroll
      for (int s = 0; s < 8; ++s) ob[s] = *(LAS u32x4*)(smem + L_OA + li * 528 + s * 64 + lg * 16);
      __builtin_amdgcn_sched_barrier(0);
      f32x4 d = {0.f, 0.f, 0.f, 0.f};
#pragma unroll
      for (int s = 0; s < 8; ++s) d = mfma16(wf[s], ob[s], d);
      __builtin_amdgcn_raw_buffer_store_b128(u32x4{__float_as_uint(d[0]), tag, __float_as_uint(d[1]), tag}, rs_ws, o_put, 0, AUX_SC1);
      __builtin_amdgcn_raw_buffer_store_b128(u32x4{__float_as_uint(d[2]), tag, __float_as_uint(d[3]), tag}, rs_ws, o_put, 1024, AUX_SC1);
    } else if (rho < 72) {
      poll4<4 * 2048>(p, rs_ws, o_get, tag);
      float s0 = (__uint_as_float(p[0]) + __uint_as_float(p[2])) + (__uint_as_float(p[4]) + __uint_as_float(p[6]));
      float s1 = (__uint_as_float(p[1]) + __uint_as_float(p[3])) + (__uint_as_float(p[5]) + __uint_as_float(p[7]));
      s0 = xrow_sum(s0); s1 = xrow_sum(s1);
      __builtin_amdgcn_raw_buffer_store_b32(pkbf(s0, s1), rs_out, o_put, 0, 0);
      if (rho == 0 && t == 0) __builtin_amdgcn_raw_buffer_store_b32(tag, rs_ws, WS_EPOCH, 0, AUX_SC1);
    }
    asm volatile("s_waitcnt vmcnt(0)" ::: "memory");
    return;
  }

  const int sw = wave;
  const unsigned tb = (unsigned)__builtin_amdgcn_s_memrealtime();
  const bool isq = b >= 8;
  if (isq) {
    const __amdgpu_buffer_rsrc_t rs_w = __builtin_amdgcn_make_buffer_rsrc((void*)wq, 0, DM * NQ * 2, RSRC_W3);
    const unsigned vw = (unsigned)(lane >> 2) * (NQ * 2) + (unsigned)(lane & 3) * 16 + (unsigned)(kvh * 256 + 32 * (b - 8)) * 2;
    int cnt = 0;
#pragma unroll 1
    for (int i = sw; i < 36; i += NW) {
      if (cnt >= KWIN) PACE(KWIN);
      dma16<1>(lds0 + L_WQ + i * 1024, vw, rs_w, (unsigned)i * (16 * NQ * 2));
      if (++cnt == KWIN) __syncthreads();
    }
    static_assert(KWIN <= 4, "every streaming wave must reach the barrier inside the weight loop");
  }
  const size_t kvoff = ((size_t)(b * HKV + kvh) * SK + (SK - WIN)) * (DH * 2);
  const __amdgpu_buffer_rsrc_t rs_k = __builtin_amdgcn_make_buffer_rsrc((void*)(kc + kvoff), 0, WIN * DH * 2, RSRC_W3);
  const __amdgpu_buffer_rsrc_t rs_v = __builtin_amdgcn_make_buffer_rsrc((void*)(vc + kvoff), 0, WIN * DH * 2, RSRC_W3);
  const unsigned voff = (unsigned)lane * 16 + (unsigned)sw * (NL * 1024);
  const unsigned ph = (unsigned)sw * 4096;
#define SOFF(u) (((u) * 1024 + ph) & (NL * 1024 - 1))
  u32x4 K[NL], V[NL], qA, qB;
  float y[NL], m = -INFINITY, mc = 0.f, l = 0.f, f = 0.f;
  const int hA = li >> 3;
  constexpr float C2 = 0.088388347648318440f * 1.4426950408889634f;
  const unsigned hm = (lane & 1) ? 0xffff0000u : 0x0000ffffu;
  const int rsel = (lane & 7) >> 1;
  const unsigned mk0 = rsel == 0 ? hm : 0u, mk1 = rsel == 1 ? hm : 0u, mk2 = rsel == 2 ? hm : 0u, mk3 = rsel == 3 ? hm : 0u;
  f32x4 acc = {0.f, 0.f, 0.f, 0.f};
  unsigned tnow = tb;
#define PV(u) do { const unsigned pp = pkbf(y[u], y[u]); acc = mfma16(V[u], u32x4{pp & mk0, pp & mk1, pp & mk2, pp & mk3}, acc); } while (0)
  sfor<2 * NL>([&](auto ic) __attribute__((always_inline)) {
    constexpr int i = decltype(ic)::value;
    if constexpr (i < KWIN) PACE(KWIN);
    else {
      PACE(VWIN);
      if (tnow - tb < (unsigned)((i - 1 + TOFF) * SLOT_NS / 10)) PACE(KWIN);
      __builtin_amdgcn_sched_barrier(0);
    }
    if constexpr (i == KWIN - 1) {
      if (isq && lane == 0) __hip_atomic_fetch_add((LAS unsigned*)(smem + L_FLAG + 12), 1u, __ATOMIC_RELAXED, __HIP_MEMORY_SCOPE_WORKGROUP);
    }
    if constexpr (i < NL) K[i] = __builtin_amdgcn_raw_buffer_load_b128(rs_k, voff, SOFF(i), AUX_NT);
    else V[i - NL] = __builtin_amdgcn_raw_buffer_load_b128(rs_v, voff, SOFF(i - NL), AUX_NT);
    __builtin_amdgcn_sched_barrier(0);
    tnow = (unsigned)__builtin_amdgcn_s_memrealtime();
    __builtin_amdgcn_sched_barrier(0);
    if constexpr (i == KWIN - 1) {
      if (!isq) __syncthreads();
    }
    if constexpr (i == 2 * NL - 1) {
      if (lane == 0) __hip_atomic_fetch_add((LAS unsigned*)(smem + L_FLAG + 20), 1u, __ATOMIC_RELAXED, __HIP_MEMORY_SCOPE_WORKGROUP);
      __builtin_amdgcn_sched_barrier(0);
    }
    if constexpr (i == QS) {
      while (flag[0] == 0u) __builtin_amdgcn_s_sleep(1);
      asm volatile("" ::: "memory");
      qA = *(LAS u32x4*)(smem + L_Q + hA * 256 + li * 16);
      qB = *(LAS u32x4*)(smem + L_Q + (hA ^ 1) * 256 + li * 16);
    }
    if constexpr (i >= QS && i < ES) {
#pragma unroll
      for (int u = 2 * (i - QS); u < 2 * (i - QS) + 2; ++u) {
        float xa = 0.f, xb = 0.f;
#pragma unroll
        for (int k = 0; k < 4; ++k) { xa = dot2(K[u][k], qA[k], xa); xb = dot2(K[u][k], qB[k], xb); }
        float t = xa + dpp<ROR8>(xb);
        t += dpp<XOR1>(t);
        t += dpp<XOR2>(t);
        t += dpp<HMIRROR>(t);
        y[u] = t;
        m = fmaxf(m, t);
        asm volatile("" : "+v"(y[u]), "+v"(m));
      }
    }
    if constexpr (i == ES) mc = xrow_max(m) * C2;
    if constexpr (i >= ES && i < PS) {
#pragma unroll
      for (int u = (NL / 4) * (i - ES); u < (NL / 4) * (i - ES + 1); ++u) { y[u] = __builtin_amdgcn_exp2f(fmaf(y[u], C2, -mc)); l += y[u]; }
      asm volatile("" : "+v"(l));
    }
    if constexpr (i == PS - 1) {
      l = xrow_sum(l);
      *(LAS f32x2*)(smem + L_ML + hA * (NW * 8) + sw * 8) = f32x2{mc, l};
      asm volatile("s_waitcnt lgkmcnt(0)" ::: "memory");
      if (lane == 0) __hip_atomic_fetch_add((LAS unsigned*)(smem + L_FLAG + 4), 1u, __ATOMIC_RELAXED, __HIP_MEMORY_SCOPE_WORKGROUP);
    }
    if constexpr (i >= PS) {
#pragma unroll
      for (int u = npv_after(i - 1); u < npv_after(i); ++u) PV(u);
      asm volatile("" : "+v"(acc));
    }
    if constexpr (i == FIN_AT) {
      __builtin_amdgcn_sched_barrier(0);
      while (flag[1] != (unsigned)NW) __builtin_amdgcn_s_sleep(1);
      asm volatile("" ::: "memory");
      f32x4 ml[NW / 2];
#pragma unroll
      for (int w = 0; w < NW / 2; ++w) ml[w] = *(LAS f32x4*)(smem + L_ML + hA * (NW * 8) + w * 16);
      float M = fmaxf(ml[0][0], ml[0][2]);
#pragma unroll
      for (int w = 1; w < NW / 2; ++w) M = fmaxf(M, fmaxf(ml[w][0], ml[w][2]));
      float L = 0.f;
#pragma unroll
      for (int w = 0; w < NW / 2; ++w) {
        L = fmaf(ml[w][1], __builtin_amdgcn_exp2f(ml[w][0] - M), L);
        L = fmaf(ml[w][3], __builtin_amdgcn_exp2f(ml[w][2] - M), L);
      }
      f = __builtin_amdgcn_exp2f(mc - M) / L;
      asm volatile("" : "+v"(f));
    }
  });
  __builtin_amdgcn_sched_barrier(0);
#pragma unroll
  for (int u = npv_after(2 * NL - 1); u < NL; ++u) PV(u);
  *(LAS f32x4*)(smem + L_RED + sw * 1024 + lane * 16) = acc * f;
  __syncthreads();
}

extern "C" void kernel_launch(void* const* d_in, const int* in_sizes, int n_in, void* d_out, int out_size,
                              void* d_ws, size_t ws_size, hipStream_t stream) {
  fused_decode_kernel<<<256, NT, 0, stream>>>((const unsigned char*)d_in[0], (const unsigned char*)d_in[1],
                                             (const unsigned char*)d_in[2], (const unsigned char*)d_in[3],
                                             (const unsigned char*)d_in[4], (unsigned char*)d_out,
                                             (unsigned char*)d_ws + ((ws_size - WS_BYTES - (8u << 20)) & ~(size_t)0x1fffff));
}
```

```cpp
#define WQP_ 0
#include <hip/hip_runtime.h>
#include <hip/hip_bf16.h>
#include <stdint.h>
#include <utility>

#define LAS __attribute__((address_space(3)))
typedef unsigned u32x4 __attribute__((ext_vector_type(4)));
typedef unsigned u32x2 __attribute__((ext_vector_type(2)));
typedef float f32x4 __attribute__((ext_vector_type(4)));
typedef float f32x2 __attribute__((ext_vector_type(2)));
typedef short s16x4 __attribute__((ext_vector_type(4)));
typedef __bf16 bf16x8 __attribute__((ext_vector_type(8)));
typedef __bf16 bf16x2 __attribute__((ext_vector_type(2)));

#define KW_ 3
#define VW_ 5
#define SL_ 288
#define TO_ 5
#define WOP_ 1
#define MAP_ 1
#define G2_ 0
#define G2D_ 0
namespace {

constexpr int NB = 16, DM = 576, HKV = 16, SK = 4096, DH = 128, WIN = 1024, NQ = 4096;
constexpr int NW = 8, NS = 4, NT = (NW + NS) * 64;
constexpr int NL = 256 / NW;

constexpr int KWIN = KW_, VWIN = VW_;
constexpr int SLOT_NS = SL_, TOFF = TO_;
constexpr int QS = 26;
constexpr int FIN_AT = 58;
constexpr int AUX_NT = 2, AUX_SC1 = 16;
constexpr int RSRC_W3 = 0x00020000;
constexpr unsigned OOB = 0x7ffffff0u;

constexpr unsigned WS_EPOCH = 0;
constexpr unsigned WS_QG = 4096;
constexpr unsigned WS_OG = WS_QG + 256 * 1024;
constexpr unsigned WS_PG = WS_OG + 256 * 1024;
constexpr unsigned WS_BYTES = WS_PG + 36 * 16 * 2 * 1024;

constexpr int L_WQ = 0;
constexpr int L_X = L_WQ + 36864;
constexpr int L_WO = L_X + 18432;
constexpr int L_RED = L_WO + 32768;
constexpr int L_OA = L_RED + NW * 1024;
constexpr int L_Q = L_OA + 16 * 528;
constexpr int L_ML = L_Q + 512;
constexpr int L_FLAG = L_ML + NW * 16;
constexpr int L_BYTES = L_FLAG + 32;

constexpr unsigned SPIN_CAP = 1u << 18;

template <class F, int... I>
__device__ __forceinline__ void sfor_impl(F&& f, std::integer_sequence<int, I...>) { (f(std::integral_constant<int, I>{}), ...); }
template <int N, class F>
__device__ __forceinline__ void sfor(F&& f) { sfor_impl(f, std::make_integer_sequence<int, N>{}); }
constexpr int ES = QS + NL / 2, PS = ES + 4;
constexpr int npv_after(int slot) {
  int n = 0;
  for (int i = PS; i <= slot; ++i)
    for (int k = 0; k < 2; ++k)
      if (n <= i - NL - VWIN) ++n;
  return n;
}

__device__ __forceinline__ float dot2(unsigned k, unsigned q, float acc) {
  return __builtin_amdgcn_fdot2_f32_bf16(__builtin_bit_cast(bf16x2, k), __builtin_bit_cast(bf16x2, q), acc, false);
}
__device__ __forceinline__ unsigned pkbf(float lo, float hi) {
  f32x2 f = {lo, hi};
  return __builtin_bit_cast(unsigned, __builtin_convertvector(f, bf16x2));
}
template <int CTRL> __device__ __forceinline__ float dpp(float x) {
  return __builtin_bit_cast(float, __builtin_amdgcn_mov_dpp(__builtin_bit_cast(int, x), CTRL, 0xf, 0xf, true));
}
constexpr int XOR1 = 0xB1, XOR2 = 0x4E, HMIRROR = 0x141, ROR8 = 0x128;
__device__ __forceinline__ float xrow_max(float x) {
  auto s = __builtin_amdgcn_permlane16_swap(__float_as_uint(x), __float_as_uint(x), false, false);
  x = fmaxf(__uint_as_float(s[0]), __uint_as_float(s[1]));
  auto t = __builtin_amdgcn_permlane32_swap(__float_as_uint(x), __float_as_uint(x), false, false);
  return fmaxf(__uint_as_float(t[0]), __uint_as_float(t[1]));
}
__device__ __forceinline__ float xrow_sum(float x) {
  auto s = __builtin_amdgcn_permlane16_swap(__float_as_uint(x), __float_as_uint(x), false, false);
  x = __uint_as_float(s[0]) + __uint_as_float(s[1]);
  auto t = __builtin_amdgcn_permlane32_swap(__float_as_uint(x), __float_as_uint(x), false, false);
  return __uint_as_float(t[0]) + __uint_as_float(t[1]);
}
__device__ __forceinline__ f32x4 mfma16(u32x4 a, u32x4 b, f32x4 c) {
  return __builtin_amdgcn_mfma_f32_16x16x32_bf16(__builtin_bit_cast(bf16x8, a), __builtin_bit_cast(bf16x8, b), c, 0, 0, 0);
}
template <int NTP>
__device__ __forceinline__ void dma16(unsigned lds, unsigned voff, __amdgpu_buffer_rsrc_t rs, unsigned soff) {
  if constexpr (NTP == 1)
    asm volatile("s_mov_b32 m0, %0\n\ts_nop 0\n\tbuffer_load_dwordx4 %1, %2, %3 offen nt lds"
                 :: "s"(lds), "v"(voff), "s"(rs), "s"(soff) : "memory");
  else
    asm volatile("s_mov_b32 m0, %0\n\ts_nop 0\n\tbuffer_load_dwordx4 %1, %2, %3 offen lds"
                 :: "s"(lds), "v"(voff), "s"(rs), "s"(soff) : "memory");
}
__device__ __forceinline__ u32x2 trread(LAS unsigned char* p) {
  s16x4 v = __builtin_amdgcn_ds_read_tr16_b64_v4i16((LAS s16x4*)p);
  return __builtin_bit_cast(u32x2, v);
}
__device__ __forceinline__ unsigned wo_src(int lane) {
  const int row = ((lane >> 4) & 1) * 4 + ((lane >> 3) & 1) * 2 + (lane >> 5);
  return (unsigned)row * (DM * 2) + (unsigned)(lane & 7) * 16;
}
template <int STRIDE>
__device__ __forceinline__ void poll4(unsigned (&p)[8], __amdgpu_buffer_rsrc_t rs, unsigned off, unsigned tag) {
  unsigned cap = SPIN_CAP, t0, t1, t2;
#define PL(b) "buffer_load_dwordx4 v[" #b ":" #b "+3], %[off], %[rs], 0 offen sc1\n\t" \
              "buffer_load_dwordx4 v[" #b "+4:" #b "+7], %[off], %[rs], %[s1] offen sc1\n\t" \
              "buffer_load_dwordx4 v[" #b "+8:" #b "+11], %[off], %[rs], %[s2] offen sc1\n\t" \
              "buffer_load_dwordx4 v[" #b "+12:" #b "+15], %[off], %[rs], %[s3] offen sc1\n\t"
#define PC(b) "s_waitcnt vmcnt(4)\n\t" \
              "v_bitop3_b32 %[t0], v[" #b "+1], v[" #b "+3], %[tag] bitop3:0x7e\n\t" \
              "v_bitop3_b32 %[t1], v[" #b "+5], v[" #b "+7], %[tag] bitop3:0x7e\n\t" \
              "v_bitop3_b32 %[t2], v[" #b "+9], v[" #b "+11], %[tag] bitop3:0x7e\n\t" \
              "v_or3_b32 %[t0], %[t0], %[t1], %[t2]\n\t" \
              "v_bitop3_b32 %[t1], v[" #b "+13], v[" #b "+15], %[tag] bitop3:0x7e\n\t" \
              "v_or_b32 %[t0], %[t0], %[t1]\n\t" \
              "v_cmp_ne_u32 vcc, 0, %[t0]\n\t"
#define PM(b) "v_mov_b32 %[p0], v[" #b "]\n\tv_mov_b32 %[p1], v[" #b "+2]\n\tv_mov_b32 %[p2], v[" #b "+4]\n\tv_mov_b32 %[p3], v[" #b "+6]\n\t" \
              "v_mov_b32 %[p4], v[" #b "+8]\n\tv_mov_b32 %[p5], v[" #b "+10]\n\tv_mov_b32 %[p6], v[" #b "+12]\n\tv_mov_b32 %[p7], v[" #b "+14]\n\t"
  asm volatile(
      PL(136)
      "s_sleep %[dl]\n"
      "1:\n\t"
      PL(152)
      PC(136)
      "s_cbranch_vccz 2f\n\t"
      "s_sub_u32 %[cap], %[cap], 1\n\t"
      "s_cbranch_scc1 2f\n\t"
      PL(136)
      PC(152)
      "s_cbranch_vccnz 1b\n\t"
      PM(152)
      "s_branch 3f\n"
      "2:\n\t"
      PM(136)
      "3:\n"
      : [p0] "=&v"(p[0]), [p1] "=&v"(p[1]), [p2] "=&v"(p[2]), [p3] "=&v"(p[3]), [p4] "=&v"(p[4]), [p5] "=&v"(p[5]), [p6] "=&v"(p[6]), [p7] "=&v"(p[7]),
        [t0] "=&v"(t0), [t1] "=&v"(t1), [t2] "=&v"(t2), [cap] "+s"(cap)
      : [off] "v"(off), [rs] "s"(rs), [tag] "v"(tag), [s1] "s"(STRIDE), [s2] "s"(2 * STRIDE), [s3] "s"(3 * STRIDE), [dl] "i"(8)
      : "memory", "vcc", "scc",
        "v136", "v137", "v138", "v139", "v140", "v141", "v142", "v143", "v144", "v145", "v146", "v147", "v148", "v149", "v150", "v151",
        "v152", "v153", "v154", "v155", "v156", "v157", "v158", "v159", "v160", "v161", "v162", "v163", "v164", "v165", "v166", "v167");
#undef PL
#undef PC
#undef PM
}

}

extern "C" __global__ void __launch_bounds__(NT, (NW + NS) / 4)
fused_decode_kernel(const unsigned char* __restrict__ x, const unsigned char* __restrict__ kc,
                    const unsigned char* __restrict__ vc, const unsigned char* __restrict__ wq,
                    const unsigned char* __restrict__ wo, unsigned char* out, unsigned char* ws) {
  __shared__ __attribute__((aligned(16))) unsigned char smem_[L_BYTES];
  LAS unsigned char* smem = (LAS unsigned char*)smem_;
  const unsigned lds0 = (unsigned)(size_t)smem;
  const int tid = threadIdx.x, lane = tid & 63;
  const int wave = __builtin_amdgcn_readfirstlane(tid >> 6);
  const int bid = blockIdx.x, jj = bid >> 3, b = jj & 15, kvh = 2 * ((bid + (MAP_ ? (b >> 1) : 0)) & 7) + (jj >> 4);
  const int li = lane & 15, lg = lane >> 4;
  volatile LAS unsigned* flag = (volatile LAS unsigned*)(smem + L_FLAG);
#define PACE(n) do { __builtin_amdgcn_sched_barrier(0); asm volatile("s_waitcnt vmcnt(%0)" :: "i"((n) - 1) : "memory"); } while (0)

  if (wave >= NW) {
    const int t = wave - NW;
    const __amdgpu_buffer_rsrc_t rs_ws = __builtin_amdgcn_make_buffer_rsrc(ws, 0, WS_BYTES, RSRC_W3);
    if (t == 0) { flag[0] = 0u; flag[1] = 0u; flag[3] = 0u; flag[4] = 0u; flag[5] = 0u; }
    __builtin_amdgcn_s_setprio(3);
    if (t != 0) __syncthreads();
    if (t == 0) {
      if (b >= 8) {
        const __amdgpu_buffer_rsrc_t rs_x = __builtin_amdgcn_make_buffer_rsrc((void*)x, 0, NB * DM * 2, RSRC_W3);
        const unsigned vx = (unsigned)(lane >> 2) * (DM * 2) + (unsigned)(lane & 3) * 16;
#pragma unroll 1
        for (int s = 0; s < 18; ++s) dma16<0>(lds0 + L_X + s * 1024, vx, rs_x, (unsigned)s * 64);
      }
      unsigned epoch = __builtin_amdgcn_raw_buffer_load_b32(rs_ws, WS_EPOCH, 0, AUX_SC1);
      __builtin_amdgcn_sched_barrier(0);
      __syncthreads();
      asm volatile("s_waitcnt vmcnt(0)" : "+v"(epoch) :: "memory");
      const unsigned tg = epoch + 1u;
      flag[2] = tg;
      if (b >= 8) {
        while (flag[3] != (unsigned)NW) __builtin_amdgcn_s_sleep(1);
        asm volatile("" ::: "memory");
        f32x4 a0 = {0.f, 0.f, 0.f, 0.f}, a1 = {0.f, 0.f, 0.f, 0.f};
        LAS unsigned char* pw = smem + L_WQ + (8 * lg + ((lane >> 2) & 3)) * 64 + (lane & 3) * 8;
        LAS unsigned char* px = smem + L_X + li * 64 + lg * 16;
#pragma unroll 6
        for (int s = 0; s < 18; ++s) {
          const u32x2 w0 = trread(pw + s * 2048), w1 = trread(pw + s * 2048 + 256);
          const u32x2 w2 = trread(pw + s * 2048 + 32), w3 = trread(pw + s * 2048 + 256 + 32);
          const u32x4 xa = *(LAS u32x4*)(px + s * 1024);
          a0 = mfma16(xa, u32x4{w0[0], w0[1], w1[0], w1[1]}, a0);
          a1 = mfma16(xa, u32x4{w2[0], w2[1], w3[0], w3[1]}, a1);
        }
        LAS unsigned* tq = (LAS unsigned*)(smem + L_OA);
#pragma unroll
        for (int r = 0; r < 4; ++r) tq[(4 * lg + r) * 16 + li] = pkbf(a0[r], a1[r]);
        asm volatile("s_waitcnt lgkmcnt(0)" ::: "memory");
        const unsigned qo = WS_QG + (unsigned)(kvh * 16 + (lane >> 3)) * 1024 + (unsigned)(b - 8) * 128 + (unsigned)(lane & 7) * 16;
#pragma unroll
        for (int k = 0; k < 2; ++k) {
          const u32x2 v = *(LAS u32x2*)(tq + k * 128 + lane * 2);
          __builtin_amdgcn_raw_buffer_store_b128(u32x4{v[0], tg, v[1], tg}, rs_ws, qo, k * 8192, AUX_SC1);
        }
        __builtin_amdgcn_s_sleep(32);
      } else {
        __builtin_amdgcn_s_sleep(96);
      }
      const unsigned qi = WS_QG + (unsigned)(kvh * 16 + b) * 1024 + (unsigned)lane * 16;
      u32x4 qr;
      for (unsigned spins = 0;; ++spins) {
        asm volatile("" ::: "memory");
        qr = __builtin_amdgcn_raw_buffer_load_b128(rs_ws, qi, 0, AUX_SC1);
        if (__builtin_amdgcn_ballot_w64(((qr[1] ^ tg) | (qr[3] ^ tg)) != 0u) == 0ull || spins > SPIN_CAP) break;
        __builtin_amdgcn_s_sleep(4);
      }
      {
        LAS unsigned* ql = (LAS unsigned*)(smem + L_Q) + 16 * (lane >> 3) + (lane & 7);
        ql[0] = __builtin_amdgcn_perm(qr[2], qr[0], 0x05040100u);
        ql[8] = __builtin_amdgcn_perm(qr[2], qr[0], 0x07060302u);
      }
      asm volatile("s_waitcnt lgkmcnt(0)" ::: "memory");
      flag[0] = 1u;
    }
    while (flag[0] == 0u) __builtin_amdgcn_s_sleep(16);
    asm volatile("" ::: "memory");
    const unsigned tag = flag[2];
    const __amdgpu_buffer_rsrc_t rs_out = __builtin_amdgcn_make_buffer_rsrc(out, 0, NB * DM * 2, RSRC_W3);
    u32x4 wf[8];
    LAS unsigned char* pwf = smem + ((lane >> 2) & 3) * 1024 + lg * 128 + ((t ^ (lane >> 2)) & 3) * 32 + (lane & 3) * 8;
#define LOADWF(dst, base) _Pragma("unroll") for (int s = 0; s < 8; ++s) { const int ro = ((s & 1) * 128 + 32 * (s >> 1)) * 128; \
        const u32x2 w0 = trread(pwf + (base) + ro), w1 = trread(pwf + (base) + ro + 512); dst[s] = u32x4{w0[0], w0[1], w1[0], w1[1]}; }
    const bool st1 = b < 8 || b == 13;
    const int cs = b < 8 ? b : 8;
    const int kv1 = 2 * (bid & 7) + (jj >> 4);
    const int rho = (b - 8) * 16 + kvh, T = rho >> 1, hf = rho & 1;
    unsigned o_pub = WS_OG + (unsigned)(kvh * 16 + b) * 1024 + (unsigned)lane * 16;
    unsigned o_get = st1 ? WS_OG + (unsigned)(kv1 * 16 + 4 * t) * 1024 + (unsigned)lane * 16
                         : WS_PG + (unsigned)((T * 16 + lg) * 2 + hf) * 1024 + (unsigned)(16 * t + li) * 16;
    unsigned o_put = st1 ? WS_PG + (unsigned)(((4 * cs + t) * 16 + kv1) * 2) * 1024 + (unsigned)lane * 16
                         : (lg == 0 ? (unsigned)(li * DM + 16 * T + 4 * t + 2 * hf) * 2 : OOB);
    asm volatile("" : "+v"(o_pub), "+v"(o_get), "+v"(o_put));
    if (st1 && t > 0) {
      const __amdgpu_buffer_rsrc_t rs_wo = __builtin_amdgcn_make_buffer_rsrc((void*)wo, 0, NQ * DM * 2, RSRC_W3);
      const unsigned vwo = wo_src(lane);
      while (flag[5] != (unsigned)NW) __builtin_amdgcn_s_sleep(2);
#pragma unroll 1
      for (int i = t - 1; i < 32; i += 3)
        dma16<WOP_>(lds0 + L_WO + i * 1024, vwo ^ (unsigned)((i & 3) * 32), rs_wo, (unsigned)(kv1 * 256 + 8 * i) * (DM * 2) + (unsigned)cs * 128);
    }
    __syncthreads();
    if (t == 0) {
      f32x4 pr[NW];
#pragma unroll
      for (int w = 0; w < NW; ++w) pr[w] = *(LAS f32x4*)(smem + L_RED + w * 1024 + lane * 16);
      __builtin_amdgcn_sched_barrier(0);
#pragma unroll
      for (int s = 1; s < NW; s *= 2)
#pragma unroll
        for (int w = 0; w < NW; w += 2 * s) pr[w] += pr[w + s];
      const f32x4 o = pr[0];
      __builtin_amdgcn_raw_buffer_store_b128(u32x4{pkbf(o[0], o[1]), tag, pkbf(o[2], o[3]), tag}, rs_ws, o_pub, 0, AUX_SC1);
    }
    unsigned p[8];
    if (st1) {
      if (t > 0) {
        asm volatile("s_waitcnt vmcnt(0)" ::: "memory");
        if (lane == 0) __hip_atomic_fetch_add((LAS unsigned*)(smem + L_FLAG + 16), 1u, __ATOMIC_RELAXED, __HIP_MEMORY_SCOPE_WORKGROUP);
      }
      while (flag[4] != 3u) __builtin_amdgcn_s_sleep(1);
      asm volatile("" ::: "memory");
      LOADWF(wf, L_WO)
      __builtin_amdgcn_sched_barrier(0);
      poll4<1024>(p, rs_ws, o_get, tag);
#pragma unroll
      for (int i = 0; i < 4; ++i) *(LAS u32x2*)(smem + L_OA + (4 * t + i) * 528 + lane * 8) = u32x2{p[2 * i], p[2 * i + 1]};
      __syncthreads();
      u32x4 ob[8];
#pragma unroll
      for (int s = 0; s < 8; ++s) ob[s] = *(LAS u32x4*)(smem + L_OA + li * 528 + s * 64 + lg * 16);
      __builtin_amdgcn_sched_barrier(0);
      f32x4 d = {0.f, 0.f, 0.f, 0.f};
#pragma unroll
      for (int s = 0; s < 8; ++s) d = mfma16(wf[s], ob[s], d);
      __builtin_amdgcn_raw_buffer_store_b128(u32x4{__float_as_uint(d[0]), tag, __float_as_uint(d[1]), tag}, rs_ws, o_put, 0, AUX_SC1);
      __builtin_amdgcn_raw_buffer_store_b128(u32x4{__float_as_uint(d[2]), tag, __float_as_uint(d[3]), tag}, rs_ws, o_put, 1024, AUX_SC1);
    } else if (rho < 72) {
      poll4<4 * 2048>(p, rs_ws, o_get, tag);
      float s0 = (__uint_as_float(p[0]) + __uint_as_float(p[2])) + (__uint_as_float(p[4]) + __uint_as_float(p[6]));
      float s1 = (__uint_as_float(p[1]) + __uint_as_float(p[3])) + (__uint_as_float(p[5]) + __uint_as_float(p[7]));
      s0 = xrow_sum(s0); s1 = xrow_sum(s1);
      __builtin_amdgcn_raw_buffer_store_b32(pkbf(s0, s1), rs_out, o_put, 0, 0);
      if (rho == 0 && t == 0) __builtin_amdgcn_raw_buffer_store_b32(tag, rs_ws, WS_EPOCH, 0, AUX_SC1);
    }
    asm volatile("s_waitcnt vmcnt(0)" ::: "memory");
    return;
  }

  const int sw = wave;
  const unsigned tb = (unsigned)__builtin_amdgcn_s_memrealtime();
  const bool isq = b >= 8;
  if (isq) {
    const __amdgpu_buffer_rsrc_t rs_w = __builtin_amdgcn_make_buffer_rsrc((void*)wq, 0, DM * NQ * 2, RSRC_W3);
    const unsigned vw = (unsigned)(lane >> 2) * (NQ * 2) + (unsigned)(lane & 3) * 16 + (unsigned)(kvh * 256 + 32 * (b - 8)) * 2;
    int cnt = 0;
#pragma unroll 1
    for (int i = sw; i < 36; i += NW) {
      if (cnt >= KWIN) PACE(KWIN);
      dma16<WQP_>(lds0 + L_WQ + i * 1024, vw, rs_w, (unsigned)i * (16 * NQ * 2));
      if (++cnt == KWIN) __syncthreads();
    }
    static_assert(KWIN <= 4, "every streaming wave must reach the barrier inside the weight loop");
  }
  const size_t kvoff = ((size_t)(b * HKV + kvh) * SK + (SK - WIN)) * (DH * 2);
  const __amdgpu_buffer_rsrc_t rs_k = __builtin_amdgcn_make_buffer_rsrc((void*)(kc + kvoff), 0, WIN * DH * 2, RSRC_W3);
  const __amdgpu_buffer_rsrc_t rs_v = __builtin_amdgcn_make_buffer_rsrc((void*)(vc + kvoff), 0, WIN * DH * 2, RSRC_W3);
  const unsigned voff = (unsigned)lane * 16 + (unsigned)sw * (NL * 1024);
  const unsigned ph = (unsigned)sw * 4096;
#define SOFF(u) (((u) * 1024 + ph) & (NL * 1024 - 1))
  u32x4 K[NL], V[NL], qA, qB;
  float y[NL], m = -INFINITY, mc = 0.f, l = 0.f, f = 0.f;
  const int hA = li >> 3;
  constexpr float C2 = 0.088388347648318440f * 1.4426950408889634f;
  const unsigned hm = (lane & 1) ? 0xffff0000u : 0x0000ffffu;
  const int rsel = (lane & 7) >> 1;
  const unsigned mk0 = rsel == 0 ? hm : 0u, mk1 = rsel == 1 ? hm : 0u, mk2 = rsel == 2 ? hm : 0u, mk3 = rsel == 3 ? hm : 0u;
  f32x4 acc = {0.f, 0.f, 0.f, 0.f};
  unsigned tnow = tb;
#define PV(u) do { const unsigned pp = pkbf(y[u], y[u]); acc = mfma16(V[u], u32x4{pp & mk0, pp & mk1, pp & mk2, pp & mk3}, acc); } while (0)
  sfor<2 * NL>([&](auto ic) __attribute__((always_inline)) {
    constexpr int i = decltype(ic)::value;
    if constexpr (i < KWIN) PACE(KWIN);
    else {
      PACE(VWIN);
      if (tnow - tb < (unsigned)((i - 1 + TOFF) * SLOT_NS / 10)) PACE(KWIN);
      __builtin_amdgcn_sched_barrier(0);
    }
    if constexpr (i == KWIN - 1) {
      if (isq && lane == 0) __hip_atomic_fetch_add((LAS unsigned*)(smem + L_FLAG + 12), 1u, __ATOMIC_RELAXED, __HIP_MEMORY_SCOPE_WORKGROUP);
    }
    if constexpr (i < NL) K[i] = __builtin_amdgcn_raw_buffer_load_b128(rs_k, voff, SOFF(i), AUX_NT);
    else V[i - NL] = __builtin_amdgcn_raw_buffer_load_b128(rs_v, voff, SOFF(i - NL), AUX_NT);
    __builtin_amdgcn_sched_barrier(0);
    tnow = (unsigned)__builtin_amdgcn_s_memrealtime();
    __builtin_amdgcn_sched_barrier(0);
    if constexpr (i == KWIN - 1) {
      if (!isq) __syncthreads();
    }
    if constexpr (i == 2 * NL - 1) {
      if (lane == 0) __hip_atomic_fetch_add((LAS unsigned*)(smem + L_FLAG + 20), 1u, __ATOMIC_RELAXED, __HIP_MEMORY_SCOPE_WORKGROUP);
      __builtin_amdgcn_sched_barrier(0);
    }
    if constexpr (i == QS) {
      while (flag[0] == 0u) __builtin_amdgcn_s_sleep(1);
      asm volatile("" ::: "memory");
      qA = *(LAS u32x4*)(smem + L_Q + hA * 256 + li * 16);
      qB = *(LAS u32x4*)(smem + L_Q + (hA ^ 1) * 256 + li * 16);
    }
    if constexpr (i >= QS && i < ES) {
#pragma unroll
      for (int u = 2 * (i - QS); u < 2 * (i - QS) + 2; ++u) {
        float xa = 0.f, xb = 0.f;
#pragma unroll
        for (int k = 0; k < 4; ++k) { xa = dot2(K[u][k], qA[k], xa); xb = dot2(K[u][k], qB[k], xb); }
        float t = xa + dpp<ROR8>(xb);
        t += dpp<XOR1>(t);
        t += dpp<XOR2>(t);
        t += dpp<HMIRROR>(t);
        y[u] = t;
        m = fmaxf(m, t);
        asm volatile("" : "+v"(y[u]), "+v"(m));
      }
    }
    if constexpr (i == ES) mc = xrow_max(m) * C2;
    if constexpr (i >= ES && i < PS) {
#pragma unroll
      for (int u = (NL / 4) * (i - ES); u < (NL / 4) * (i - ES + 1); ++u) { y[u] = __builtin_amdgcn_exp2f(fmaf(y[u], C2, -mc)); l += y[u]; }
      asm volatile("" : "+v"(l));
    }
    if constexpr (i == PS - 1) {
      l = xrow_sum(l);
      *(LAS f32x2*)(smem + L_ML + hA * (NW * 8) + sw * 8) = f32x2{mc, l};
      asm volatile("s_waitcnt lgkmcnt(0)" ::: "memory");
      if (lane == 0) __hip_atomic_fetch_add((LAS unsigned*)(smem + L_FLAG + 4), 1u, __ATOMIC_RELAXED, __HIP_MEMORY_SCOPE_WORKGROUP);
    }
    if constexpr (i >= PS) {
#pragma unroll
      for (int u = npv_after(i - 1); u < npv_after(i); ++u) PV(u);
      asm volatile("" : "+v"(acc));
    }
    if constexpr (i == FIN_AT) {
      __builtin_amdgcn_sched_barrier(0);
      while (flag[1] != (unsigned)NW) __builtin_amdgcn_s_sleep(1);
      asm volatile("" ::: "memory");
      f32x4 ml[NW / 2];
#pragma unroll
      for (int w = 0; w < NW / 2; ++w) ml[w] = *(LAS f32x4*)(smem + L_ML + hA * (NW * 8) + w * 16);
      float M = fmaxf(ml[0][0], ml[0][2]);
#pragma unroll
      for (int w = 1; w < NW / 2; ++w) M = fmaxf(M, fmaxf(ml[w][0], ml[w][2]));
      float L = 0.f;
#pragma unroll
      for (int w = 0; w < NW / 2; ++w) {
        L = fmaf(ml[w][1], __builtin_amdgcn_exp2f(ml[w][0] - M), L);
        L = fmaf(ml[w][3], __builtin_amdgcn_exp2f(ml[w][2] - M), L);
      }
      f = __builtin_amdgcn_exp2f(mc - M) / L;
      asm volatile("" : "+v"(f));
    }
  });
  __builtin_amdgcn_sched_barrier(0);
#pragma unroll
  for (int u = npv_after(2 * NL - 1); u < NL; ++u) PV(u);
  *(LAS f32x4*)(smem + L_RED + sw * 1024 + lane * 16) = acc * f;
  __syncthreads();
}

extern "C" void kernel_launch(void* const* d_in, const int* in_sizes, int n_in, void* d_out, int out_size,
                              void* d_ws, size_t ws_size, hipStream_t stream) {
  fused_decode_kernel<<<256, NT, 0, stream>>>((const unsigned char*)d_in[0], (const unsigned char*)d_in[1],
                                             (const unsigned char*)d_in[2], (const unsigned char*)d_in[3],
                                             (const unsigned char*)d_in[4], (unsigned char*)d_out,
                                             (unsigned char*)d_ws);
}
```

```cpp
#define G2_ 1
#define G2D_ 26
#include <hip/hip_runtime.h>
#include <hip/hip_bf16.h>
#include <stdint.h>
#include <utility>

#define LAS __attribute__((address_space(3)))
typedef unsigned u32x4 __attribute__((ext_vector_type(4)));
typedef unsigned u32x2 __attribute__((ext_vector_type(2)));
typedef float f32x4 __attribute__((ext_vector_type(4)));
typedef float f32x2 __attribute__((ext_vector_type(2)));
typedef short s16x4 __attribute__((ext_vector_type(4)));
typedef __bf16 bf16x8 __attribute__((ext_vector_type(8)));
typedef __bf16 bf16x2 __attribute__((ext_vector_type(2)));

#define KW_ 3
#define VW_ 5
#define SL_ 288
#define TO_ 5
#define WQP_ 0
#define WOP_ 0
#define MAP_ 1
namespace {

constexpr int NB = 16, DM = 576, HKV = 16, SK = 4096, DH = 128, WIN = 1024, NQ = 4096;
constexpr int NW = 8, NS = 4, NT = (NW + NS) * 64;
constexpr int NL = 256 / NW;

constexpr int KWIN = KW_, VWIN = VW_;
constexpr int SLOT_NS = SL_, TOFF = TO_;
constexpr int QS = 26;
constexpr int FIN_AT = 58;
constexpr int AUX_NT = 2, AUX_SC1 = 16;
constexpr int RSRC_W3 = 0x00020000;
constexpr unsigned OOB = 0x7ffffff0u;

constexpr unsigned WS_EPOCH = 0;
constexpr unsigned WS_QG = 4096;
constexpr unsigned WS_OG = WS_QG + 256 * 1024;
constexpr unsigned WS_PG = WS_OG + 256 * 1024;
constexpr unsigned WS_BYTES = WS_PG + 36 * 16 * 2 * 1024;

constexpr int L_WQ = 0;
constexpr int L_X = L_WQ + 36864;
constexpr int L_WO = L_X + 18432;
constexpr int L_RED = L_WO + 32768;
constexpr int L_OA = L_RED + NW * 1024;
constexpr int L_Q = L_OA + 16 * 528;
constexpr int L_ML = L_Q + 512;
constexpr int L_FLAG = L_ML + NW * 16;
constexpr int L_BYTES = L_FLAG + 32;

constexpr unsigned SPIN_CAP = 1u << 18;

template <class F, int... I>
__device__ __forceinline__ void sfor_impl(F&& f, std::integer_sequence<int, I...>) { (f(std::integral_constant<int, I>{}), ...); }
template <int N, class F>
__device__ __forceinline__ void sfor(F&& f) { sfor_impl(f, std::make_integer_sequence<int, N>{}); }
constexpr int ES = QS + NL / 2, PS = ES + 4;
constexpr int npv_after(int slot) {
  int n = 0;
  for (int i = PS; i <= slot; ++i)
    for (int k = 0; k < 2; ++k)
      if (n <= i - NL - VWIN) ++n;
  return n;
}

__device__ __forceinline__ float dot2(unsigned k, unsigned q, float acc) {
  return __builtin_amdgcn_fdot2_f32_bf16(__builtin_bit_cast(bf16x2, k), __builtin_bit_cast(bf16x2, q), acc, false);
}
__device__ __forceinline__ unsigned pkbf(float lo, float hi) {
  f32x2 f = {lo, hi};
  return __builtin_bit_cast(unsigned, __builtin_convertvector(f, bf16x2));
}
template <int CTRL> __device__ __forceinline__ float dpp(float x) {
  return __builtin_bit_cast(float, __builtin_amdgcn_mov_dpp(__builtin_bit_cast(int, x), CTRL, 0xf, 0xf, true));
}
constexpr int XOR1 = 0xB1, XOR2 = 0x4E, HMIRROR = 0x141, ROR8 = 0x128;
__device__ __forceinline__ float xrow_max(float x) {
  auto s = __builtin_amdgcn_permlane16_swap(__float_as_uint(x), __float_as_uint(x), false, false);
  x = fmaxf(__uint_as_float(s[0]), __uint_as_float(s[1]));
  auto t = __builtin_amdgcn_permlane32_swap(__float_as_uint(x), __float_as_uint(x), false, false);
  return fmaxf(__uint_as_float(t[0]), __uint_as_float(t[1]));
}
__device__ __forceinline__ float xrow_sum(float x) {
  auto s = __builtin_amdgcn_permlane16_swap(__float_as_uint(x), __float_as_uint(x), false, false);
  x = __uint_as_float(s[0]) + __uint_as_float(s[1]);
  auto t = __builtin_amdgcn_permlane32_swap(__float_as_uint(x), __float_as_uint(x), false, false);
  return __uint_as_float(t[0]) + __uint_as_float(t[1]);
}
__device__ __forceinline__ f32x4 mfma16(u32x4 a, u32x4 b, f32x4 c) {
  return __builtin_amdgcn_mfma_f32_16x16x32_bf16(__builtin_bit_cast(bf16x8, a), __builtin_bit_cast(bf16x8, b), c, 0, 0, 0);
}
template <int NTP>
__device__ __forceinline__ void dma16(unsigned lds, unsigned voff, __amdgpu_buffer_rsrc_t rs, unsigned soff) {
  if constexpr (NTP == 1)
    asm volatile("s_mov_b32 m0, %0\n\ts_nop 0\n\tbuffer_load_dwordx4 %1, %2, %3 offen nt lds"
                 :: "s"(lds), "v"(voff), "s"(rs), "s"(soff) : "memory");
  else
    asm volatile("s_mov_b32 m0, %0\n\ts_nop 0\n\tbuffer_load_dwordx4 %1, %2, %3 offen lds"
                 :: "s"(lds), "v"(voff), "s"(rs), "s"(soff) : "memory");
}
__device__ __forceinline__ u32x2 trread(LAS unsigned char* p) {
  s16x4 v = __builtin_amdgcn_ds_read_tr16_b64_v4i16((LAS s16x4*)p);
  return __builtin_bit_cast(u32x2, v);
}
__device__ __forceinline__ unsigned wo_src(int lane) {
  const int row = ((lane >> 4) & 1) * 4 + ((lane >> 3) & 1) * 2 + (lane >> 5);
  return (unsigned)row * (DM * 2) + (unsigned)(lane & 7) * 16;
}
template <int STRIDE>
__device__ __forceinline__ void poll4(unsigned (&p)[8], __amdgpu_buffer_rsrc_t rs, unsigned off, unsigned tag) {
  unsigned cap = SPIN_CAP, t0, t1, t2;
#define PL(b) "buffer_load_dwordx4 v[" #b ":" #b "+3], %[off], %[rs], 0 offen sc1\n\t" \
              "buffer_load_dwordx4 v[" #b "+4:" #b "+7], %[off], %[rs], %[s1] offen sc1\n\t" \
              "buffer_load_dwordx4 v[" #b "+8:" #b "+11], %[off], %[rs], %[s2] offen sc1\n\t" \
              "buffer_load_dwordx4 v[" #b "+12:" #b "+15], %[off], %[rs], %[s3] offen sc1\n\t"
#define PC(b) "s_waitcnt vmcnt(4)\n\t" \
              "v_bitop3_b32 %[t0], v[" #b "+1], v[" #b "+3], %[tag] bitop3:0x7e\n\t" \
              "v_bitop3_b32 %[t1], v[" #b "+5], v[" #b "+7], %[tag] bitop3:0x7e\n\t" \
              "v_bitop3_b32 %[t2], v[" #b "+9], v[" #b "+11], %[tag] bitop3:0x7e\n\t" \
              "v_or3_b32 %[t0], %[t0], %[t1], %[t2]\n\t" \
              "v_bitop3_b32 %[t1], v[" #b "+13], v[" #b "+15], %[tag] bitop3:0x7e\n\t" \
              "v_or_b32 %[t0], %[t0], %[t1]\n\t" \
              "v_cmp_ne_u32 vcc, 0, %[t0]\n\t"
#define PM(b) "v_mov_b32 %[p0], v[" #b "]\n\tv_mov_b32 %[p1], v[" #b "+2]\n\tv_mov_b32 %[p2], v[" #b "+4]\n\tv_mov_b32 %[p3], v[" #b "+6]\n\t" \
              "v_mov_b32 %[p4], v[" #b "+8]\n\tv_mov_b32 %[p5], v[" #b "+10]\n\tv_mov_b32 %[p6], v[" #b "+12]\n\tv_mov_b32 %[p7], v[" #b "+14]\n\t"
  asm volatile(
      PL(136)
      "s_sleep %[dl]\n"
      "1:\n\t"
      PL(152)
      PC(136)
      "s_cbranch_vccz 2f\n\t"
      "s_sub_u32 %[cap], %[cap], 1\n\t"
      "s_cbranch_scc1 2f\n\t"
      PL(136)
      PC(152)
      "s_cbranch_vccnz 1b\n\t"
      PM(152)
      "s_branch 3f\n"
      "2:\n\t"
      PM(136)
      "3:\n"
      : [p0] "=&v"(p[0]), [p1] "=&v"(p[1]), [p2] "=&v"(p[2]), [p3] "=&v"(p[3]), [p4] "=&v"(p[4]), [p5] "=&v"(p[5]), [p6] "=&v"(p[6]), [p7] "=&v"(p[7]),
        [t0] "=&v"(t0), [t1] "=&v"(t1), [t2] "=&v"(t2), [cap] "+s"(cap)
      : [off] "v"(off), [rs] "s"(rs), [tag] "v"(tag), [s1] "s"(STRIDE), [s2] "s"(2 * STRIDE), [s3] "s"(3 * STRIDE), [dl] "i"(8)
      : "memory", "vcc", "scc",
        "v136", "v137", "v138", "v139", "v140", "v141", "v142", "v143", "v144", "v145", "v146", "v147", "v148", "v149", "v150", "v151",
        "v152", "v153", "v154", "v155", "v156", "v157", "v158", "v159", "v160", "v161", "v162", "v163", "v164", "v165", "v166", "v167");
#undef PL
#undef PC
#undef PM
}

}

extern "C" __global__ void __launch_bounds__(NT, (NW + NS) / 4)
fused_decode_kernel(const unsigned char* __restrict__ x, const unsigned char* __restrict__ kc,
                    const unsigned char* __restrict__ vc, const unsigned char* __restrict__ wq,
                    const unsigned char* __restrict__ wo, unsigned char* out, unsigned char* ws) {
  __shared__ __attribute__((aligned(16))) unsigned char smem_[L_BYTES];
  LAS unsigned char* smem = (LAS unsigned char*)smem_;
  const unsigned lds0 = (unsigned)(size_t)smem;
  const int tid = threadIdx.x, lane = tid & 63;
  const int wave = __builtin_amdgcn_readfirstlane(tid >> 6);
  const int bid = blockIdx.x, jj = bid >> 3, b = jj & 15, kvh = 2 * ((bid + (MAP_ ? (b >> 1) : 0)) & 7) + (jj >> 4);
  const int li = lane & 15, lg = lane >> 4;
  volatile LAS unsigned* flag = (volatile LAS unsigned*)(smem + L_FLAG);
#define PACE(n) do { __builtin_amdgcn_sched_barrier(0); asm volatile("s_waitcnt vmcnt(%0)" :: "i"((n) - 1) : "memory"); } while (0)

  if (wave >= NW) {
    const int t = wave - NW;
    const __amdgpu_buffer_rsrc_t rs_ws = __builtin_amdgcn_make_buffer_rsrc(ws, 0, WS_BYTES, RSRC_W3);
    if (t == 0) { flag[0] = 0u; flag[1] = 0u; flag[3] = 0u; flag[4] = 0u; flag[5] = 0u; }
    __builtin_amdgcn_s_setprio(3);
    if (t != 0) __syncthreads();
    if (t == 0) {
      if (b >= 8) {
        const __amdgpu_buffer_rsrc_t rs_x = __builtin_amdgcn_make_buffer_rsrc((void*)x, 0, NB * DM * 2, RSRC_W3);
        const unsigned vx = (unsigned)(lane >> 2) * (DM * 2) + (unsigned)(lane & 3) * 16;
#pragma unroll 1
        for (int s = 0; s < 18; ++s) dma16<0>(lds0 + L_X + s * 1024, vx, rs_x, (unsigned)s * 64);
      }
      unsigned epoch = __builtin_amdgcn_raw_buffer_load_b32(rs_ws, WS_EPOCH, 0, AUX_SC1);
      __builtin_amdgcn_sched_barrier(0);
      __syncthreads();
      asm volatile("s_waitcnt vmcnt(0)" : "+v"(epoch) :: "memory");
      const unsigned tg = epoch + 1u;
      flag[2] = tg;
      if (b >= 8) {
        while (flag[3] != (unsigned)NW) __builtin_amdgcn_s_sleep(1);
        asm volatile("" ::: "memory");
        f32x4 a0 = {0.f, 0.f, 0.f, 0.f}, a1 = {0.f, 0.f, 0.f, 0.f};
        LAS unsigned char* pw = smem + L_WQ + (8 * lg + ((lane >> 2) & 3)) * 64 + (lane & 3) * 8;
        LAS unsigned char* px = smem + L_X + li * 64 + lg * 16;
#pragma unroll 6
        for (int s = 0; s < 18; ++s) {
          const u32x2 w0 = trread(pw + s * 2048), w1 = trread(pw + s * 2048 + 256);
          const u32x2 w2 = trread(pw + s * 2048 + 32), w3 = trread(pw + s * 2048 + 256 + 32);
          const u32x4 xa = *(LAS u32x4*)(px + s * 1024);
          a0 = mfma16(xa, u32x4{w0[0], w0[1], w1[0], w1[1]}, a0);
          a1 = mfma16(xa, u32x4{w2[0], w2[1], w3[0], w3[1]}, a1);
        }
        LAS unsigned* tq = (LAS unsigned*)(smem + L_OA);
#pragma unroll
        for (int r = 0; r < 4; ++r) tq[(4 * lg + r) * 16 + li] = pkbf(a0[r], a1[r]);
        asm volatile("s_waitcnt lgkmcnt(0)" ::: "memory");
        const unsigned qo = WS_QG + (unsigned)(kvh * 16 + (lane >> 3)) * 1024 + (unsigned)(b - 8) * 128 + (unsigned)(lane & 7) * 16;
#pragma unroll
        for (int k = 0; k < 2; ++k) {
          const u32x2 v = *(LAS u32x2*)(tq + k * 128 + lane * 2);
          __builtin_amdgcn_raw_buffer_store_b128(u32x4{v[0], tg, v[1], tg}, rs_ws, qo, k * 8192, AUX_SC1);
        }
        __builtin_amdgcn_s_sleep(32);
      } else {
        __builtin_amdgcn_s_sleep(96);
      }
      const unsigned qi = WS_QG + (unsigned)(kvh * 16 + b) * 1024 + (unsigned)lane * 16;
      u32x4 qr;
      for (unsigned spins = 0;; ++spins) {
        asm volatile("" ::: "memory");
        qr = __builtin_amdgcn_raw_buffer_load_b128(rs_ws, qi, 0, AUX_SC1);
        if (__builtin_amdgcn_ballot_w64(((qr[1] ^ tg) | (qr[3] ^ tg)) != 0u) == 0ull || spins > SPIN_CAP) break;
        __builtin_amdgcn_s_sleep(4);
      }
      {
        LAS unsigned* ql = (LAS unsigned*)(smem + L_Q) + 16 * (lane >> 3) + (lane & 7);
        ql[0] = __builtin_amdgcn_perm(qr[2], qr[0], 0x05040100u);
        ql[8] = __builtin_amdgcn_perm(qr[2], qr[0], 0x07060302u);
      }
      asm volatile("s_waitcnt lgkmcnt(0)" ::: "memory");
      flag[0] = 1u;
    }
    while (flag[0] == 0u) __builtin_amdgcn_s_sleep(16);
    asm volatile("" ::: "memory");
    const unsigned tag = flag[2];
    const __amdgpu_buffer_rsrc_t rs_out = __builtin_amdgcn_make_buffer_rsrc(out, 0, NB * DM * 2, RSRC_W3);
    u32x4 wf[8];
    LAS unsigned char* pwf = smem + ((lane >> 2) & 3) * 1024 + lg * 128 + ((t ^ (lane >> 2)) & 3) * 32 + (lane & 3) * 8;
#define LOADWF(dst, base) _Pragma("unroll") for (int s = 0; s < 8; ++s) { const int ro = ((s & 1) * 128 + 32 * (s >> 1)) * 128; \
        const u32x2 w0 = trread(pwf + (base) + ro), w1 = trread(pwf + (base) + ro + 512); dst[s] = u32x4{w0[0], w0[1], w1[0], w1[1]}; }
    const bool st1 = b < 8 || b == 13;
    const int cs = b < 8 ? b : 8;
    const int kv1 = 2 * (bid & 7) + (jj >> 4);
    const int rho = (b - 8) * 16 + kvh, T = rho >> 1, hf = rho & 1;
    unsigned o_pub = WS_OG + (unsigned)(kvh * 16 + b) * 1024 + (unsigned)lane * 16;
    unsigned o_get = st1 ? WS_OG + (unsigned)(kv1 * 16 + 4 * t) * 1024 + (unsigned)lane * 16
                         : WS_PG + (unsigned)((T * 16 + lg) * 2 + hf) * 1024 + (unsigned)(16 * t + li) * 16;
    unsigned o_put = st1 ? WS_PG + (unsigned)(((4 * cs + t) * 16 + kv1) * 2) * 1024 + (unsigned)lane * 16
                         : (lg == 0 ? (unsigned)(li * DM + 16 * T + 4 * t + 2 * hf) * 2 : OOB);
    asm volatile("" : "+v"(o_pub), "+v"(o_get), "+v"(o_put));
    if (st1 && t > 0) {
      const __amdgpu_buffer_rsrc_t rs_wo = __builtin_amdgcn_make_buffer_rsrc((void*)wo, 0, NQ * DM * 2, RSRC_W3);
      const unsigned vwo = wo_src(lane);
      while (flag[5] != (unsigned)NW) __builtin_amdgcn_s_sleep(2);
#pragma unroll 1
      for (int i = t - 1; i < 32; i += 3)
        dma16<WOP_>(lds0 + L_WO + i * 1024, vwo ^ (unsigned)((i & 3) * 32), rs_wo, (unsigned)(kv1 * 256 + 8 * i) * (DM * 2) + (unsigned)cs * 128);
    }
    __syncthreads();
    if (t == 0) {
      f32x4 pr[NW];
#pragma unroll
      for (int w = 0; w < NW; ++w) pr[w] = *(LAS f32x4*)(smem + L_RED + w * 1024 + lane * 16);
      __builtin_amdgcn_sched_barrier(0);
#pragma unroll
      for (int s = 1; s < NW; s *= 2)
#pragma unroll
        for (int w = 0; w < NW; w += 2 * s) pr[w] += pr[w + s];
      const f32x4 o = pr[0];
      __builtin_amdgcn_raw_buffer_store_b128(u32x4{pkbf(o[0], o[1]), tag, pkbf(o[2], o[3]), tag}, rs_ws, o_pub, 0, AUX_SC1);
    }
    unsigned p[8];
    if (st1) {
      if (t > 0) {
        asm volatile("s_waitcnt vmcnt(0)" ::: "memory");
        if (lane == 0) __hip_atomic_fetch_add((LAS unsigned*)(smem + L_FLAG + 16), 1u, __ATOMIC_RELAXED, __HIP_MEMORY_SCOPE_WORKGROUP);
      }
      while (flag[4] != 3u) __builtin_amdgcn_s_sleep(1);
      asm volatile("" ::: "memory");
      LOADWF(wf, L_WO)
      __builtin_amdgcn_sched_barrier(0);
      poll4<1024>(p, rs_ws, o_get, tag);
#pragma unroll
      for (int i = 0; i < 4; ++i) *(LAS u32x2*)(smem + L_OA + (4 * t + i) * 528 + lane * 8) = u32x2{p[2 * i], p[2 * i + 1]};
      __syncthreads();
      u32x4 ob[8];
#pragma unroll
      for (int s = 0; s < 8; ++s) ob[s] = *(LAS u32x4*)(smem + L_OA + li * 528 + s * 64 + lg * 16);
      __builtin_amdgcn_sched_barrier(0);
      f32x4 d = {0.f, 0.f, 0.f, 0.f};
#pragma unroll
      for (int s = 0; s < 8; ++s) d = mfma16(wf[s], ob[s], d);
      __builtin_amdgcn_raw_buffer_store_b128(u32x4{__float_as_uint(d[0]), tag, __float_as_uint(d[1]), tag}, rs_ws, o_put, 0, AUX_SC1);
      __builtin_amdgcn_raw_buffer_store_b128(u32x4{__float_as_uint(d[2]), tag, __float_as_uint(d[3]), tag}, rs_ws, o_put, 1024, AUX_SC1);
    } else if (rho < 72) {
      if (t == 1) {
        for (unsigned spins = 0;; ++spins) {
          asm volatile("" ::: "memory");
          const u32x2 g = __builtin_amdgcn_raw_buffer_load_b64(rs_ws, WS_OG + (unsigned)(kv1 * 16 + li) * 1024, 0, AUX_SC1);
          if (__builtin_amdgcn_ballot_w64(g[1] != tag) == 0ull || spins >= SPIN_CAP) break;
          __builtin_amdgcn_s_sleep(1);
        }
        if (G2D_) __builtin_amdgcn_s_sleep(G2D_);
        flag[4] = 1u;
      } else {
        while (flag[4] == 0u) __builtin_amdgcn_s_sleep(1);
      }
      poll4<4 * 2048>(p, rs_ws, o_get, tag);
      float s0 = (__uint_as_float(p[0]) + __uint_as_float(p[2])) + (__uint_as_float(p[4]) + __uint_as_float(p[6]));
      float s1 = (__uint_as_float(p[1]) + __uint_as_float(p[3])) + (__uint_as_float(p[5]) + __uint_as_float(p[7]));
      s0 = xrow_sum(s0); s1 = xrow_sum(s1);
      __builtin_amdgcn_raw_buffer_store_b32(pkbf(s0, s1), rs_out, o_put, 0, 0);
      if (rho == 0 && t == 0) __builtin_amdgcn_raw_buffer_store_b32(tag, rs_ws, WS_EPOCH, 0, AUX_SC1);
    }
    asm volatile("s_waitcnt vmcnt(0)" ::: "memory");
    return;
  }

  const int sw = wave;
  const unsigned tb = (unsigned)__builtin_amdgcn_s_memrealtime();
  const bool isq = b >= 8;
  if (isq) {
    const __amdgpu_buffer_rsrc_t rs_w = __builtin_amdgcn_make_buffer_rsrc((void*)wq, 0, DM * NQ * 2, RSRC_W3);
    const unsigned vw = (unsigned)(lane >> 2) * (NQ * 2) + (unsigned)(lane & 3) * 16 + (unsigned)(kvh * 256 + 32 * (b - 8)) * 2;
    int cnt = 0;
#pragma unroll 1
    for (int i = sw; i < 36; i += NW) {
      if (cnt >= KWIN) PACE(KWIN);
      dma16<WQP_>(lds0 + L_WQ + i * 1024, vw, rs_w, (unsigned)i * (16 * NQ * 2));
      if (++cnt == KWIN) __syncthreads();
    }
    static_assert(KWIN <= 4, "every streaming wave must reach the barrier inside the weight loop");
  }
  const size_t kvoff = ((size_t)(b * HKV + kvh) * SK + (SK - WIN)) * (DH * 2);
  const __amdgpu_buffer_rsrc_t rs_k = __builtin_amdgcn_make_buffer_rsrc((void*)(kc + kvoff), 0, WIN * DH * 2, RSRC_W3);
  const __amdgpu_buffer_rsrc_t rs_v = __builtin_amdgcn_make_buffer_rsrc((void*)(vc + kvoff), 0, WIN * DH * 2, RSRC_W3);
  const unsigned voff = (unsigned)lane * 16 + (unsigned)sw * (NL * 1024);
  const unsigned ph = (unsigned)sw * 4096;
#define SOFF(u) (((u) * 1024 + ph) & (NL * 1024 - 1))
  u32x4 K[NL], V[NL], qA, qB;
  float y[NL], m = -INFINITY, mc = 0.f, l = 0.f, f = 0.f;
  const int hA = li >> 3;
  constexpr float C2 = 0.088388347648318440f * 1.4426950408889634f;
  const unsigned hm = (lane & 1) ? 0xffff0000u : 0x0000ffffu;
  const int rsel = (lane & 7) >> 1;
  const unsigned mk0 = rsel == 0 ? hm : 0u, mk1 = rsel == 1 ? hm : 0u, mk2 = rsel == 2 ? hm : 0u, mk3 = rsel == 3 ? hm : 0u;
  f32x4 acc = {0.f, 0.f, 0.f, 0.f};
  unsigned tnow = tb;
#define PV(u) do { const unsigned pp = pkbf(y[u], y[u]); acc = mfma16(V[u], u32x4{pp & mk0, pp & mk1, pp & mk2, pp & mk3}, acc); } while (0)
  sfor<2 * NL>([&](auto ic) __attribute__((always_inline)) {
    constexpr int i = decltype(ic)::value;
    if constexpr (i < KWIN) PACE(KWIN);
    else {
      PACE(VWIN);
      if (tnow - tb < (unsigned)((i - 1 + TOFF) * SLOT_NS / 10)) PACE(KWIN);
      __builtin_amdgcn_sched_barrier(0);
    }
    if constexpr (i == KWIN - 1) {
      if (isq && lane == 0) __hip_atomic_fetch_add((LAS unsigned*)(smem + L_FLAG + 12), 1u, __ATOMIC_RELAXED, __HIP_MEMORY_SCOPE_WORKGROUP);
    }
    if constexpr (i < NL) K[i] = __builtin_amdgcn_raw_buffer_load_b128(rs_k, voff, SOFF(i), AUX_NT);
    else V[i - NL] = __builtin_amdgcn_raw_buffer_load_b128(rs_v, voff, SOFF(i - NL), AUX_NT);
    __builtin_amdgcn_sched_barrier(0);
    tnow = (unsigned)__builtin_amdgcn_s_memrealtime();
    __builtin_amdgcn_sched_barrier(0);
    if constexpr (i == KWIN - 1) {
      if (!isq) __syncthreads();
    }
    if constexpr (i == 2 * NL - 1) {
      if (lane == 0) __hip_atomic_fetch_add((LAS unsigned*)(smem + L_FLAG + 20), 1u, __ATOMIC_RELAXED, __HIP_MEMORY_SCOPE_WORKGROUP);
      __builtin_amdgcn_sched_barrier(0);
    }
    if constexpr (i == QS) {
      while (flag[0] == 0u) __builtin_amdgcn_s_sleep(1);
      asm volatile("" ::: "memory");
      qA = *(LAS u32x4*)(smem + L_Q + hA * 256 + li * 16);
      qB = *(LAS u32x4*)(smem + L_Q + (hA ^ 1) * 256 + li * 16);
    }
    if constexpr (i >= QS && i < ES) {
#pragma unroll
      for (int u = 2 * (i - QS); u < 2 * (i - QS) + 2; ++u) {
        float xa = 0.f, xb = 0.f;
#pragma unroll
        for (int k = 0; k < 4; ++k) { xa = dot2(K[u][k], qA[k], xa); xb = dot2(K[u][k], qB[k], xb); }
        float t = xa + dpp<ROR8>(xb);
        t += dpp<XOR1>(t);
        t += dpp<XOR2>(t);
        t += dpp<HMIRROR>(t);
        y[u] = t;
        m = fmaxf(m, t);
        asm volatile("" : "+v"(y[u]), "+v"(m));
      }
    }
    if constexpr (i == ES) mc = xrow_max(m) * C2;
    if constexpr (i >= ES && i < PS) {
#pragma unroll
      for (int u = (NL / 4) * (i - ES); u < (NL / 4) * (i - ES + 1); ++u) { y[u] = __builtin_amdgcn_exp2f(fmaf(y[u], C2, -mc)); l += y[u]; }
      asm volatile("" : "+v"(l));
    }
    if constexpr (i == PS - 1) {
      l = xrow_sum(l);
      *(LAS f32x2*)(smem + L_ML + hA * (NW * 8) + sw * 8) = f32x2{mc, l};
      asm volatile("s_waitcnt lgkmcnt(0)" ::: "memory");
      if (lane == 0) __hip_atomic_fetch_add((LAS unsigned*)(smem + L_FLAG + 4), 1u, __ATOMIC_RELAXED, __HIP_MEMORY_SCOPE_WORKGROUP);
    }
    if constexpr (i >= PS) {
#pragma unroll
      for (int u = npv_after(i - 1); u < npv_after(i); ++u) PV(u);
      asm volatile("" : "+v"(acc));
    }
    if constexpr (i == FIN_AT) {
      __builtin_amdgcn_sched_barrier(0);
      while (flag[1] != (unsigned)NW) __builtin_amdgcn_s_sleep(1);
      asm volatile("" ::: "memory");
      f32x4 ml[NW / 2];
#pragma unroll
      for (int w = 0; w < NW / 2; ++w) ml[w] = *(LAS f32x4*)(smem + L_ML + hA * (NW * 8) + w * 16);
      float M = fmaxf(ml[0][0], ml[0][2]);
#pragma unroll
      for (int w = 1; w < NW / 2; ++w) M = fmaxf(M, fmaxf(ml[w][0], ml[w][2]));
      float L = 0.f;
#pragma unroll
      for (int w = 0; w < NW / 2; ++w) {
        L = fmaf(ml[w][1], __builtin_amdgcn_exp2f(ml[w][0] - M), L);
        L = fmaf(ml[w][3], __builtin_amdgcn_exp2f(ml[w][2] - M), L);
      }
      f = __builtin_amdgcn_exp2f(mc - M) / L;
      asm volatile("" : "+v"(f));
    }
  });
  __builtin_amdgcn_sched_barrier(0);
#pragma unroll
  for (int u = npv_after(2 * NL - 1); u < NL; ++u) PV(u);
  *(LAS f32x4*)(smem + L_RED + sw * 1024 + lane * 16) = acc * f;
  __syncthreads();
}

extern "C" void kernel_launch(void* const* d_in, const int* in_sizes, int n_in, void* d_out, int out_size,
                              void* d_ws, size_t ws_size, hipStream_t stream) {
  fused_decode_kernel<<<256, NT, 0, stream>>>((const unsigned char*)d_in[0], (const unsigned char*)d_in[1],
                                             (const unsigned char*)d_in[2], (const unsigned char*)d_in[3],
                                             (const unsigned char*)d_in[4], (unsigned char*)d_out,
                                             (unsigned char*)d_ws);
}
```

```cpp
#define SL_ 304
#define TO_ 3
#include <hip/hip_runtime.h>
#include <hip/hip_bf16.h>
#include <stdint.h>
#include <utility>

#define LAS __attribute__((address_space(3)))
typedef unsigned u32x4 __attribute__((ext_vector_type(4)));
typedef unsigned u32x2 __attribute__((ext_vector_type(2)));
typedef float f32x4 __attribute__((ext_vector_type(4)));
typedef float f32x2 __attribute__((ext_vector_type(2)));
typedef short s16x4 __attribute__((ext_vector_type(4)));
typedef __bf16 bf16x8 __attribute__((ext_vector_type(8)));
typedef __bf16 bf16x2 __attribute__((ext_vector_type(2)));

#define KW_ 3
#define VW_ 5
#define WQP_ 0
#define WOP_ 0
#define MAP_ 1
#define G2_ 0
#define G2D_ 0
namespace {

constexpr int NB = 16, DM = 576, HKV = 16, SK = 4096, DH = 128, WIN = 1024, NQ = 4096;
constexpr int NW = 8, NS = 4, NT = (NW + NS) * 64;
constexpr int NL = 256 / NW;

constexpr int KWIN = KW_, VWIN = VW_;
constexpr int SLOT_NS = SL_, TOFF = TO_;
constexpr int QS = 26;
constexpr int FIN_AT = 58;
constexpr int AUX_NT = 2, AUX_SC1 = 16;
constexpr int RSRC_W3 = 0x00020000;
constexpr unsigned OOB = 0x7ffffff0u;

constexpr unsigned WS_EPOCH = 0;
constexpr unsigned WS_QG = 4096;
constexpr unsigned WS_OG = WS_QG + 256 * 1024;
constexpr unsigned WS_PG = WS_OG + 256 * 1024;
constexpr unsigned WS_BYTES = WS_PG + 36 * 16 * 2 * 1024;

constexpr int L_WQ = 0;
constexpr int L_X = L_WQ + 36864;
constexpr int L_WO = L_X + 18432;
constexpr int L_RED = L_WO + 32768;
constexpr int L_OA = L_RED + NW * 1024;
constexpr int L_Q = L_OA + 16 * 528;
constexpr int L_ML = L_Q + 512;
constexpr int L_FLAG = L_ML + NW * 16;
constexpr int L_BYTES = L_FLAG + 32;

constexpr unsigned SPIN_CAP = 1u << 18;

template <class F, int... I>
__device__ __forceinline__ void sfor_impl(F&& f, std::integer_sequence<int, I...>) { (f(std::integral_constant<int, I>{}), ...); }
template <int N, class F>
__device__ __forceinline__ void sfor(F&& f) { sfor_impl(f, std::make_integer_sequence<int, N>{}); }
constexpr int ES = QS + NL / 2, PS = ES + 4;
constexpr int npv_after(int slot) {
  int n = 0;
  for (int i = PS; i <= slot; ++i)
    for (int k = 0; k < 2; ++k)
      if (n <= i - NL - VWIN) ++n;
  return n;
}

__device__ __forceinline__ float dot2(unsigned k, unsigned q, float acc) {
  return __builtin_amdgcn_fdot2_f32_bf16(__builtin_bit_cast(bf16x2, k), __builtin_bit_cast(bf16x2, q), acc, false);
}
__device__ __forceinline__ unsigned pkbf(float lo, float hi) {
  f32x2 f = {lo, hi};
  return __builtin_bit_cast(unsigned, __builtin_convertvector(f, bf16x2));
}
template <int CTRL> __device__ __forceinline__ float dpp(float x) {
  return __builtin_bit_cast(float, __builtin_amdgcn_mov_dpp(__builtin_bit_cast(int, x), CTRL, 0xf, 0xf, true));
}
constexpr int XOR1 = 0xB1, XOR2 = 0x4E, HMIRROR = 0x141, ROR8 = 0x128;
__device__ __forceinline__ float xrow_max(float x) {
  auto s = __builtin_amdgcn_permlane16_swap(__float_as_uint(x), __float_as_uint(x), false, false);
  x = fmaxf(__uint_as_float(s[0]), __uint_as_float(s[1]));
  auto t = __builtin_amdgcn_permlane32_swap(__float_as_uint(x), __float_as_uint(x), false, false);
  return fmaxf(__uint_as_float(t[0]), __uint_as_float(t[1]));
}
__device__ __forceinline__ float xrow_sum(float x) {
  auto s = __builtin_amdgcn_permlane16_swap(__float_as_uint(x), __float_as_uint(x), false, false);
  x = __uint_as_float(s[0]) + __uint_as_float(s[1]);
  auto t = __builtin_amdgcn_permlane32_swap(__float_as_uint(x), __float_as_uint(x), false, false);
  return __uint_as_float(t[0]) + __uint_as_float(t[1]);
}
__device__ __forceinline__ f32x4 mfma16(u32x4 a, u32x4 b, f32x4 c) {
  return __builtin_amdgcn_mfma_f32_16x16x32_bf16(__builtin_bit_cast(bf16x8, a), __builtin_bit_cast(bf16x8, b), c, 0, 0, 0);
}
template <int NTP>
__device__ __forceinline__ void dma16(unsigned lds, unsigned voff, __amdgpu_buffer_rsrc_t rs, unsigned soff) {
  if constexpr (NTP == 1)
    asm volatile("s_mov_b32 m0, %0\n\ts_nop 0\n\tbuffer_load_dwordx4 %1, %2, %3 offen nt lds"
                 :: "s"(lds), "v"(voff), "s"(rs), "s"(soff) : "memory");
  else
    asm volatile("s_mov_b32 m0, %0\n\ts_nop 0\n\tbuffer_load_dwordx4 %1, %2, %3 offen lds"
                 :: "s"(lds), "v"(voff), "s"(rs), "s"(soff) : "memory");
}
__device__ __forceinline__ u32x2 trread(LAS unsigned char* p) {
  s16x4 v = __builtin_amdgcn_ds_read_tr16_b64_v4i16((LAS s16x4*)p);
  return __builtin_bit_cast(u32x2, v);
}
__device__ __forceinline__ unsigned wo_src(int lane) {
  const int row = ((lane >> 4) & 1) * 4 + ((lane >> 3) & 1) * 2 + (lane >> 5);
  return (unsigned)row * (DM * 2) + (unsigned)(lane & 7) * 16;
}
template <int STRIDE>
__device__ __forceinline__ void poll4(unsigned (&p)[8], __amdgpu_buffer_rsrc_t rs, unsigned off, unsigned tag) {
  unsigned cap = SPIN_CAP, t0, t1, t2;
#define PL(b) "buffer_load_dwordx4 v[" #b ":" #b "+3], %[off], %[rs], 0 offen sc1\n\t" \
              "buffer_load_dwordx4 v[" #b "+4:" #b "+7], %[off], %[rs], %[s1] offen sc1\n\t" \
              "buffer_load_dwordx4 v[" #b "+8:" #b "+11], %[off], %[rs], %[s2] offen sc1\n\t" \
              "buffer_load_dwordx4 v[" #b "+12:" #b "+15], %[off], %[rs], %[s3] offen sc1\n\t"
#define PC(b) "s_waitcnt vmcnt(4)\n\t" \
              "v_bitop3_b32 %[t0], v[" #b "+1], v[" #b "+3], %[tag] bitop3:0x7e\n\t" \
              "v_bitop3_b32 %[t1], v[" #b "+5], v[" #b "+7], %[tag] bitop3:0x7e\n\t" \
              "v_bitop3_b32 %[t2], v[" #b "+9], v[" #b "+11], %[tag] bitop3:0x7e\n\t" \
              "v_or3_b32 %[t0], %[t0], %[t1], %[t2]\n\t" \
              "v_bitop3_b32 %[t1], v[" #b "+13], v[" #b "+15], %[tag] bitop3:0x7e\n\t" \
              "v_or_b32 %[t0], %[t0], %[t1]\n\t" \
              "v_cmp_ne_u32 vcc, 0, %[t0]\n\t"
#define PM(b) "v_mov_b32 %[p0], v[" #b "]\n\tv_mov_b32 %[p1], v[" #b "+2]\n\tv_mov_b32 %[p2], v[" #b "+4]\n\tv_mov_b32 %[p3], v[" #b "+6]\n\t" \
              "v_mov_b32 %[p4], v[" #b "+8]\n\tv_mov_b32 %[p5], v[" #b "+10]\n\tv_mov_b32 %[p6], v[" #b "+12]\n\tv_mov_b32 %[p7], v[" #b "+14]\n\t"
  asm volatile(
      PL(136)
      "s_sleep %[dl]\n"
      "1:\n\t"
      PL(152)
      PC(136)
      "s_cbranch_vccz 2f\n\t"
      "s_sub_u32 %[cap], %[cap], 1\n\t"
      "s_cbranch_scc1 2f\n\t"
      PL(136)
      PC(152)
      "s_cbranch_vccnz 1b\n\t"
      PM(152)
      "s_branch 3f\n"
      "2:\n\t"
      PM(136)
      "3:\n"
      : [p0] "=&v"(p[0]), [p1] "=&v"(p[1]), [p2] "=&v"(p[2]), [p3] "=&v"(p[3]), [p4] "=&v"(p[4]), [p5] "=&v"(p[5]), [p6] "=&v"(p[6]), [p7] "=&v"(p[7]),
        [t0] "=&v"(t0), [t1] "=&v"(t1), [t2] "=&v"(t2), [cap] "+s"(cap)
      : [off] "v"(off), [rs] "s"(rs), [tag] "v"(tag), [s1] "s"(STRIDE), [s2] "s"(2 * STRIDE), [s3] "s"(3 * STRIDE), [dl] "i"(8)
      : "memory", "vcc", "scc",
        "v136", "v137", "v138", "v139", "v140", "v141", "v142", "v143", "v144", "v145", "v146", "v147", "v148", "v149", "v150", "v151",
        "v152", "v153", "v154", "v155", "v156", "v157", "v158", "v159", "v160", "v161", "v162", "v163", "v164", "v165", "v166", "v167");
#undef PL
#undef PC
#undef PM
}

}

extern "C" __global__ void __launch_bounds__(NT, (NW + NS) / 4)
fused_decode_kernel(const unsigned char* __restrict__ x, const unsigned char* __restrict__ kc,
                    const unsigned char* __restrict__ vc, const unsigned char* __restrict__ wq,
                    const unsigned char* __restrict__ wo, unsigned char* out, unsigned char* ws) {
  __shared__ __attribute__((aligned(16))) unsigned char smem_[L_BYTES];
  LAS unsigned char* smem = (LAS unsigned char*)smem_;
  const unsigned lds0 = (unsigned)(size_t)smem;
  const int tid = threadIdx.x, lane = tid & 63;
  const int wave = __builtin_amdgcn_readfirstlane(tid >> 6);
  const int bid = blockIdx.x, jj = bid >> 3, b = jj & 15, kvh = 2 * ((bid + (MAP_ ? (b >> 1) : 0)) & 7) + (jj >> 4);
  const int li = lane & 15, lg = lane >> 4;
  volatile LAS unsigned* flag = (volatile LAS unsigned*)(smem + L_FLAG);
#define PACE(n) do { __builtin_amdgcn_sched_barrier(0); asm volatile("s_waitcnt vmcnt(%0)" :: "i"((n) - 1) : "memory"); } while (0)

  if (wave >= NW) {
    const int t = wave - NW;
    const __amdgpu_buffer_rsrc_t rs_ws = __builtin_amdgcn_make_buffer_rsrc(ws, 0, WS_BYTES, RSRC_W3);
    if (t == 0) { flag[0] = 0u; flag[1] = 0u; flag[3] = 0u; flag[4] = 0u; flag[5] = 0u; }
    __builtin_amdgcn_s_setprio(3);
    if (t != 0) __syncthreads();
    if (t == 0) {
      if (b >= 8) {
        const __amdgpu_buffer_rsrc_t rs_x = __builtin_amdgcn_make_buffer_rsrc((void*)x, 0, NB * DM * 2, RSRC_W3);
        const unsigned vx = (unsigned)(lane >> 2) * (DM * 2) + (unsigned)(lane & 3) * 16;
#pragma unroll 1
        for (int s = 0; s < 18; ++s) dma16<0>(lds0 + L_X + s * 1024, vx, rs_x, (unsigned)s * 64);
      }
      unsigned epoch = __builtin_amdgcn_raw_buffer_load_b32(rs_ws, WS_EPOCH, 0, AUX_SC1);
      __builtin_amdgcn_sched_barrier(0);
      __syncthreads();
      asm volatile("s_waitcnt vmcnt(0)" : "+v"(epoch) :: "memory");
      const unsigned tg = epoch + 1u;
      flag[2] = tg;
      if (b >= 8) {
        while (flag[3] != (unsigned)NW) __builtin_amdgcn_s_sleep(1);
        asm volatile("" ::: "memory");
        f32x4 a0 = {0.f, 0.f, 0.f, 0.f}, a1 = {0.f, 0.f, 0.f, 0.f};
        LAS unsigned char* pw = smem + L_WQ + (8 * lg + ((lane >> 2) & 3)) * 64 + (lane & 3) * 8;
        LAS unsigned char* px = smem + L_X + li * 64 + lg * 16;
#pragma unroll 6
        for (int s = 0; s < 18; ++s) {
          const u32x2 w0 = trread(pw + s * 2048), w1 = trread(pw + s * 2048 + 256);
          const u32x2 w2 = trread(pw + s * 2048 + 32), w3 = trread(pw + s * 2048 + 256 + 32);
          const u32x4 xa = *(LAS u32x4*)(px + s * 1024);
          a0 = mfma16(xa, u32x4{w0[0], w0[1], w1[0], w1[1]}, a0);
          a1 = mfma16(xa, u32x4{w2[0], w2[1], w3[0], w3[1]}, a1);
        }
        LAS unsigned* tq = (LAS unsigned*)(smem + L_OA);
#pragma unroll
        for (int r = 0; r < 4; ++r) tq[(4 * lg + r) * 16 + li] = pkbf(a0[r], a1[r]);
        asm volatile("s_waitcnt lgkmcnt(0)" ::: "memory");
        const unsigned qo = WS_QG + (unsigned)(kvh * 16 + (lane >> 3)) * 1024 + (unsigned)(b - 8) * 128 + (unsigned)(lane & 7) * 16;
#pragma unroll
        for (int k = 0; k < 2; ++k) {
          const u32x2 v = *(LAS u32x2*)(tq + k * 128 + lane * 2);
          __builtin_amdgcn_raw_buffer_store_b128(u32x4{v[0], tg, v[1], tg}, rs_ws, qo, k * 8192, AUX_SC1);
        }
        __builtin_amdgcn_s_sleep(32);
      } else {
        __builtin_amdgcn_s_sleep(96);
      }
      const unsigned qi = WS_QG + (unsigned)(kvh * 16 + b) * 1024 + (unsigned)lane * 16;
      u32x4 qr;
      for (unsigned spins = 0;; ++spins) {
        asm volatile("" ::: "memory");
        qr = __builtin_amdgcn_raw_buffer_load_b128(rs_ws, qi, 0, AUX_SC1);
        if (__builtin_amdgcn_ballot_w64(((qr[1] ^ tg) | (qr[3] ^ tg)) != 0u) == 0ull || spins > SPIN_CAP) break;
        __builtin_amdgcn_s_sleep(4);
      }
      {
        LAS unsigned* ql = (LAS unsigned*)(smem + L_Q) + 16 * (lane >> 3) + (lane & 7);
        ql[0] = __builtin_amdgcn_perm(qr[2], qr[0], 0x05040100u);
        ql[8] = __builtin_amdgcn_perm(qr[2], qr[0], 0x07060302u);
      }
      asm volatile("s_waitcnt lgkmcnt(0)" ::: "memory");
      flag[0] = 1u;
    }
    while (flag[0] == 0u) __builtin_amdgcn_s_sleep(16);
    asm volatile("" ::: "memory");
    const unsigned tag = flag[2];
    const __amdgpu_buffer_rsrc_t rs_out = __builtin_amdgcn_make_buffer_rsrc(out, 0, NB * DM * 2, RSRC_W3);
    u32x4 wf[8];
    LAS unsigned char* pwf = smem + ((lane >> 2) & 3) * 1024 + lg * 128 + ((t ^ (lane >> 2)) & 3) * 32 + (lane & 3) * 8;
#define LOADWF(dst, base) _Pragma("unroll") for (int s = 0; s < 8; ++s) { const int ro = ((s & 1) * 128 + 32 * (s >> 1)) * 128; \
        const u32x2 w0 = trread(pwf + (base) + ro), w1 = trread(pwf + (base) + ro + 512); dst[s] = u32x4{w0[0], w0[1], w1[0], w1[1]}; }
    const bool st1 = b < 8 || b == 13;
    const int cs = b < 8 ? b : 8;
    const int kv1 = 2 * (bid & 7) + (jj >> 4);
    const int rho = (b - 8) * 16 + kvh, T = rho >> 1, hf = rho & 1;
    unsigned o_pub = WS_OG + (unsigned)(kvh * 16 + b) * 1024 + (unsigned)lane * 16;
    unsigned o_get = st1 ? WS_OG + (unsigned)(kv1 * 16 + 4 * t) * 1024 + (unsigned)lane * 16
                         : WS_PG + (unsigned)((T * 16 + lg) * 2 + hf) * 1024 + (unsigned)(16 * t + li) * 16;
    unsigned o_put = st1 ? WS_PG + (unsigned)(((4 * cs + t) * 16 + kv1) * 2) * 1024 + (unsigned)lane * 16
                         : (lg == 0 ? (unsigned)(li * DM + 16 * T + 4 * t + 2 * hf) * 2 : OOB);
    asm volatile("" : "+v"(o_pub), "+v"(o_get), "+v"(o_put));
    if (st1 && t > 0) {
      const __amdgpu_buffer_rsrc_t rs_wo = __builtin_amdgcn_make_buffer_rsrc((void*)wo, 0, NQ * DM * 2, RSRC_W3);
      const unsigned vwo = wo_src(lane);
      while (flag[5] != (unsigned)NW) __builtin_amdgcn_s_sleep(2);
#pragma unroll 1
      for (int i = t - 1; i < 32; i += 3)
        dma16<WOP_>(lds0 + L_WO + i * 1024, vwo ^ (unsigned)((i & 3) * 32), rs_wo, (unsigned)(kv1 * 256 + 8 * i) * (DM * 2) + (unsigned)cs * 128);
    }
    __syncthreads();
    if (t == 0) {
      f32x4 pr[NW];
#pragma unroll
      for (int w = 0; w < NW; ++w) pr[w] = *(LAS f32x4*)(smem + L_RED + w * 1024 + lane * 16);
      __builtin_amdgcn_sched_barrier(0);
#pragma unroll
      for (int s = 1; s < NW; s *= 2)
#pragma unroll
        for (int w = 0; w < NW; w += 2 * s) pr[w] += pr[w + s];
      const f32x4 o = pr[0];
      __builtin_amdgcn_raw_buffer_store_b128(u32x4{pkbf(o[0], o[1]), tag, pkbf(o[2], o[3]), tag}, rs_ws, o_pub, 0, AUX_SC1);
    }
    unsigned p[8];
    if (st1) {
      if (t > 0) {
        asm volatile("s_waitcnt vmcnt(0)" ::: "memory");
        if (lane == 0) __hip_atomic_fetch_add((LAS unsigned*)(smem + L_FLAG + 16), 1u, __ATOMIC_RELAXED, __HIP_MEMORY_SCOPE_WORKGROUP);
      }
      while (flag[4] != 3u) __builtin_amdgcn_s_sleep(1);
      asm volatile("" ::: "memory");
      LOADWF(wf, L_WO)
      __builtin_amdgcn_sched_barrier(0);
      poll4<1024>(p, rs_ws, o_get, tag);
#pragma unroll
      for (int i = 0; i < 4; ++i) *(LAS u32x2*)(smem + L_OA + (4 * t + i) * 528 + lane * 8) = u32x2{p[2 * i], p[2 * i + 1]};
      __syncthreads();
      u32x4 ob[8];
#pragma unroll
      for (int s = 0; s < 8; ++s) ob[s] = *(LAS u32x4*)(smem + L_OA + li * 528 + s * 64 + lg * 16);
      __builtin_amdgcn_sched_barrier(0);
      f32x4 d = {0.f, 0.f, 0.f, 0.f};
#pragma unroll
      for (int s = 0; s < 8; ++s) d = mfma16(wf[s], ob[s], d);
      __builtin_amdgcn_raw_buffer_store_b128(u32x4{__float_as_uint(d[0]), tag, __float_as_uint(d[1]), tag}, rs_ws, o_put, 0, AUX_SC1);
      __builtin_amdgcn_raw_buffer_store_b128(u32x4{__float_as_uint(d[2]), tag, __float_as_uint(d[3]), tag}, rs_ws, o_put, 1024, AUX_SC1);
    } else if (rho < 72) {
      poll4<4 * 2048>(p, rs_ws, o_get, tag);
      float s0 = (__uint_as_float(p[0]) + __uint_as_float(p[2])) + (__uint_as_float(p[4]) + __uint_as_float(p[6]));
      float s1 = (__uint_as_float(p[1]) + __uint_as_float(p[3])) + (__uint_as_float(p[5]) + __uint_as_float(p[7]));
      s0 = xrow_sum(s0); s1 = xrow_sum(s1);
      __builtin_amdgcn_raw_buffer_store_b32(pkbf(s0, s1), rs_out, o_put, 0, 0);
      if (rho == 0 && t == 0) __builtin_amdgcn_raw_buffer_store_b32(tag, rs_ws, WS_EPOCH, 0, AUX_SC1);
    }
    asm volatile("s_waitcnt vmcnt(0)" ::: "memory");
    return;
  }

  const int sw = wave;
  const unsigned tb = (unsigned)__builtin_amdgcn_s_memrealtime();
  const bool isq = b >= 8;
  if (isq) {
    const __amdgpu_buffer_rsrc_t rs_w = __builtin_amdgcn_make_buffer_rsrc((void*)wq, 0, DM * NQ * 2, RSRC_W3);
    const unsigned vw = (unsigned)(lane >> 2) * (NQ * 2) + (unsigned)(lane & 3) * 16 + (unsigned)(kvh * 256 + 32 * (b - 8)) * 2;
    int cnt = 0;
#pragma unroll 1
    for (int i = sw; i < 36; i += NW) {
      if (cnt >= KWIN) PACE(KWIN);
      dma16<WQP_>(lds0 + L_WQ + i * 1024, vw, rs_w, (unsigned)i * (16 * NQ * 2));
      if (++cnt == KWIN) __syncthreads();
    }
    static_assert(KWIN <= 4, "every streaming wave must reach the barrier inside the weight loop");
  }
  const size_t kvoff = ((size_t)(b * HKV + kvh) * SK + (SK - WIN)) * (DH * 2);
  const __amdgpu_buffer_rsrc_t rs_k = __builtin_amdgcn_make_buffer_rsrc((void*)(kc + kvoff), 0, WIN * DH * 2, RSRC_W3);
  const __amdgpu_buffer_rsrc_t rs_v = __builtin_amdgcn_make_buffer_rsrc((void*)(vc + kvoff), 0, WIN * DH * 2, RSRC_W3);
  const unsigned voff = (unsigned)lane * 16 + (unsigned)sw * (NL * 1024);
  const unsigned ph = (unsigned)sw * 4096;
#define SOFF(u) (((u) * 1024 + ph) & (NL * 1024 - 1))
  u32x4 K[NL], V[NL], qA, qB;
  float y[NL], m = -INFINITY, mc = 0.f, l = 0.f, f = 0.f;
  const int hA = li >> 3;
  constexpr float C2 = 0.088388347648318440f * 1.4426950408889634f;
  const unsigned hm = (lane & 1) ? 0xffff0000u : 0x0000ffffu;
  const int rsel = (lane & 7) >> 1;
  const unsigned mk0 = rsel == 0 ? hm : 0u, mk1 = rsel == 1 ? hm : 0u, mk2 = rsel == 2 ? hm : 0u, mk3 = rsel == 3 ? hm : 0u;
  f32x4 acc = {0.f, 0.f, 0.f, 0.f};
  unsigned tnow = tb;
#define PV(u) do { const unsigned pp = pkbf(y[u], y[u]); acc = mfma16(V[u], u32x4{pp & mk0, pp & mk1, pp & mk2, pp & mk3}, acc); } while (0)
  sfor<2 * NL>([&](auto ic) __attribute__((always_inline)) {
    constexpr int i = decltype(ic)::value;
    if constexpr (i < KWIN) PACE(KWIN);
    else {
      PACE(VWIN);
      if (tnow - tb < (unsigned)((i - 1 + TOFF) * SLOT_NS / 10)) PACE(KWIN);
      __builtin_amdgcn_sched_barrier(0);
    }
    if constexpr (i == KWIN - 1) {
      if (isq && lane == 0) __hip_atomic_fetch_add((LAS unsigned*)(smem + L_FLAG + 12), 1u, __ATOMIC_RELAXED, __HIP_MEMORY_SCOPE_WORKGROUP);
    }
    if constexpr (i < NL) K[i] = __builtin_amdgcn_raw_buffer_load_b128(rs_k, voff, SOFF(i), AUX_NT);
    else V[i - NL] = __builtin_amdgcn_raw_buffer_load_b128(rs_v, voff, SOFF(i - NL), AUX_NT);
    __builtin_amdgcn_sched_barrier(0);
    tnow = (unsigned)__builtin_amdgcn_s_memrealtime();
    __builtin_amdgcn_sched_barrier(0);
    if constexpr (i == KWIN - 1) {
      if (!isq) __syncthreads();
    }
    if constexpr (i == 2 * NL - 1) {
      if (lane == 0) __hip_atomic_fetch_add((LAS unsigned*)(smem + L_FLAG + 20), 1u, __ATOMIC_RELAXED, __HIP_MEMORY_SCOPE_WORKGROUP);
      __builtin_amdgcn_sched_barrier(0);
    }
    if constexpr (i == QS) {
      while (flag[0] == 0u) __builtin_amdgcn_s_sleep(1);
      asm volatile("" ::: "memory");
      qA = *(LAS u32x4*)(smem + L_Q + hA * 256 + li * 16);
      qB = *(LAS u32x4*)(smem + L_Q + (hA ^ 1) * 256 + li * 16);
    }
    if constexpr (i >= QS && i < ES) {
#pragma unroll
      for (int u = 2 * (i - QS); u < 2 * (i - QS) + 2; ++u) {
        float xa = 0.f, xb = 0.f;
#pragma unroll
        for (int k = 0; k < 4; ++k) { xa = dot2(K[u][k], qA[k], xa); xb = dot2(K[u][k], qB[k], xb); }
        float t = xa + dpp<ROR8>(xb);
        t += dpp<XOR1>(t);
        t += dpp<XOR2>(t);
        t += dpp<HMIRROR>(t);
        y[u] = t;
        m = fmaxf(m, t);
        asm volatile("" : "+v"(y[u]), "+v"(m));
      }
    }
    if constexpr (i == ES) mc = xrow_max(m) * C2;
    if constexpr (i >= ES && i < PS) {
#pragma unroll
      for (int u = (NL / 4) * (i - ES); u < (NL / 4) * (i - ES + 1); ++u) { y[u] = __builtin_amdgcn_exp2f(fmaf(y[u], C2, -mc)); l += y[u]; }
      asm volatile("" : "+v"(l));
    }
    if constexpr (i == PS - 1) {
      l = xrow_sum(l);
      *(LAS f32x2*)(smem + L_ML + hA * (NW * 8) + sw * 8) = f32x2{mc, l};
      asm volatile("s_waitcnt lgkmcnt(0)" ::: "memory");
      if (lane == 0) __hip_atomic_fetch_add((LAS unsigned*)(smem + L_FLAG + 4), 1u, __ATOMIC_RELAXED, __HIP_MEMORY_SCOPE_WORKGROUP);
    }
    if constexpr (i >= PS) {
#pragma unroll
      for (int u = npv_after(i - 1); u < npv_after(i); ++u) PV(u);
      asm volatile("" : "+v"(acc));
    }
    if constexpr (i == FIN_AT) {
      __builtin_amdgcn_sched_barrier(0);
      while (flag[1] != (unsigned)NW) __builtin_amdgcn_s_sleep(1);
      asm volatile("" ::: "memory");
      f32x4 ml[NW / 2];
#pragma unroll
      for (int w = 0; w < NW / 2; ++w) ml[w] = *(LAS f32x4*)(smem + L_ML + hA * (NW * 8) + w * 16);
      float M = fmaxf(ml[0][0], ml[0][2]);
#pragma unroll
      for (int w = 1; w < NW / 2; ++w) M = fmaxf(M, fmaxf(ml[w][0], ml[w][2]));
      float L = 0.f;
#pragma unroll
      for (int w = 0; w < NW / 2; ++w) {
        L = fmaf(ml[w][1], __builtin_amdgcn_exp2f(ml[w][0] - M), L);
        L = fmaf(ml[w][3], __builtin_amdgcn_exp2f(ml[w][2] - M), L);
      }
      f = __builtin_amdgcn_exp2f(mc - M) / L;
      asm volatile("" : "+v"(f));
    }
  });
  __builtin_amdgcn_sched_barrier(0);
#pragma unroll
  for (int u = npv_after(2 * NL - 1); u < NL; ++u) PV(u);
  *(LAS f32x4*)(smem + L_RED + sw * 1024 + lane * 16) = acc * f;
  __syncthreads();
}

extern "C" void kernel_launch(void* const* d_in, const int* in_sizes, int n_in, void* d_out, int out_size,
                              void* d_ws, size_t ws_size, hipStream_t stream) {
  fused_decode_kernel<<<256, NT, 0, stream>>>((const unsigned char*)d_in[0], (const unsigned char*)d_in[1],
                                             (const unsigned char*)d_in[2], (const unsigned char*)d_in[3],
                                             (const unsigned char*)d_in[4], (unsigned char*)d_out,
                                             (unsigned char*)d_ws);
}
```

```cpp
#include <hip/hip_runtime.h>
#include <hip/hip_bf16.h>
#include <stdint.h>
#include <utility>

#define LAS __attribute__((address_space(3)))
typedef unsigned u32x4 __attribute__((ext_vector_type(4)));
typedef unsigned u32x2 __attribute__((ext_vector_type(2)));
typedef float f32x4 __attribute__((ext_vector_type(4)));
typedef float f32x2 __attribute__((ext_vector_type(2)));
typedef short s16x4 __attribute__((ext_vector_type(4)));
typedef __bf16 bf16x8 __attribute__((ext_vector_type(8)));
typedef __bf16 bf16x2 __attribute__((ext_vector_type(2)));

namespace {

constexpr int NB = 16, DM = 576, HKV = 16, SK = 4096, DH = 128, WIN = 1024, NQ = 4096;
constexpr int NW = 8, NS = 4, NT = (NW + NS) * 64;
constexpr int NL = 256 / NW;

constexpr int KWIN = 3, VWIN = 5;
constexpr int SLOT_NS = 294, TOFF = 5;
constexpr int QS = 26;
constexpr int FIN_AT = 58;
constexpr int AUX_NT = 2, AUX_SC1 = 16;
constexpr int RSRC_W3 = 0x00020000;
constexpr unsigned OOB = 0x7ffffff0u;

constexpr unsigned WS_EPOCH = 0;
constexpr unsigned WS_QG = 4096;
constexpr unsigned WS_OG = WS_QG + 256 * 1024;
constexpr unsigned WS_PG = WS_OG + 256 * 1024;
constexpr unsigned WS_BYTES = WS_PG + 36 * 16 * 2 * 1024;

constexpr int L_WQ = 0;
constexpr int L_X = L_WQ + 36864;
constexpr int L_WO = L_X + 18432;
constexpr int L_RED = L_WO + 32768;
constexpr int L_OA = L_RED + NW * 1024;
constexpr int L_Q = L_OA + 16 * 528;
constexpr int L_ML = L_Q + 512;
constexpr int L_FLAG = L_ML + NW * 16;
constexpr int L_BYTES = L_FLAG + 32;

constexpr unsigned SPIN_CAP = 1u << 18;

template <class F, int... I>
__device__ __forceinline__ void sfor_impl(F&& f, std::integer_sequence<int, I...>) { (f(std::integral_constant<int, I>{}), ...); }
template <int N, class F>
__device__ __forceinline__ void sfor(F&& f) { sfor_impl(f, std::make_integer_sequence<int, N>{}); }
constexpr int ES = QS + NL / 2, PS = ES + 4;
constexpr int npv_after(int slot) {
  int n = 0;
  for (int i = PS; i <= slot; ++i)
    for (int k = 0; k < 2; ++k)
      if (n <= i - NL - VWIN) ++n;
  return n;
}

__device__ __forceinline__ float dot2(unsigned k, unsigned q, float acc) {
  return __builtin_amdgcn_fdot2_f32_bf16(__builtin_bit_cast(bf16x2, k), __builtin_bit_cast(bf16x2, q), acc, false);
}
__device__ __forceinline__ unsigned pkbf(float lo, float hi) {
  f32x2 f = {lo, hi};
  return __builtin_bit_cast(unsigned, __builtin_convertvector(f, bf16x2));
}
template <int CTRL> __device__ __forceinline__ float dpp(float x) {
  return __builtin_bit_cast(float, __builtin_amdgcn_mov_dpp(__builtin_bit_cast(int, x), CTRL, 0xf, 0xf, true));
}
constexpr int XOR1 = 0xB1, XOR2 = 0x4E, HMIRROR = 0x141, ROR8 = 0x128;
__device__ __forceinline__ float xrow_max(float x) {
  auto s = __builtin_amdgcn_permlane16_swap(__float_as_uint(x), __float_as_uint(x), false, false);
  x = fmaxf(__uint_as_float(s[0]), __uint_as_float(s[1]));
  auto t = __builtin_amdgcn_permlane32_swap(__float_as_uint(x), __float_as_uint(x), false, false);
  return fmaxf(__uint_as_float(t[0]), __uint_as_float(t[1]));
}
__device__ __forceinline__ float xrow_sum(float x) {
  auto s = __builtin_amdgcn_permlane16_swap(__float_as_uint(x), __float_as_uint(x), false, false);
  x = __uint_as_float(s[0]) + __uint_as_float(s[1]);
  auto t = __builtin_amdgcn_permlane32_swap(__float_as_uint(x), __float_as_uint(x), false, false);
  return __uint_as_float(t[0]) + __uint_as_float(t[1]);
}
__device__ __forceinline__ f32x4 mfma16(u32x4 a, u32x4 b, f32x4 c) {
  return __builtin_amdgcn_mfma_f32_16x16x32_bf16(__builtin_bit_cast(bf16x8, a), __builtin_bit_cast(bf16x8, b), c, 0, 0, 0);
}
__device__ __forceinline__ void dma16(unsigned lds, unsigned voff, __amdgpu_buffer_rsrc_t rs, unsigned soff) {
  asm volatile("s_mov_b32 m0, %0\n\ts_nop 0\n\tbuffer_load_dwordx4 %1, %2, %3 offen lds"
               :: "s"(lds), "v"(voff), "s"(rs), "s"(soff) : "memory");
}
__device__ __forceinline__ u32x2 trread(LAS unsigned char* p) {
  s16x4 v = __builtin_amdgcn_ds_read_tr16_b64_v4i16((LAS s16x4*)p);
  return __builtin_bit_cast(u32x2, v);
}
__device__ __forceinline__ unsigned wo_src(int lane) {
  const int row = ((lane >> 4) & 1) * 4 + ((lane >> 3) & 1) * 2 + (lane >> 5);
  return (unsigned)row * (DM * 2) + (unsigned)(lane & 7) * 16;
}
template <int STRIDE>
__device__ __forceinline__ void poll4(unsigned (&p)[8], __amdgpu_buffer_rsrc_t rs, unsigned off, unsigned tag) {
  unsigned cap = SPIN_CAP, t0, t1, t2;
#define PL(b) "buffer_load_dwordx4 v[" #b ":" #b "+3], %[off], %[rs], 0 offen sc1\n\t" \
              "buffer_load_dwordx4 v[" #b "+4:" #b "+7], %[off], %[rs], %[s1] offen sc1\n\t" \
              "buffer_load_dwordx4 v[" #b "+8:" #b "+11], %[off], %[rs], %[s2] offen sc1\n\t" \
              "buffer_load_dwordx4 v[" #b "+12:" #b "+15], %[off], %[rs], %[s3] offen sc1\n\t"
#define PC(b) "s_waitcnt vmcnt(4)\n\t" \
              "v_bitop3_b32 %[t0], v[" #b "+1], v[" #b "+3], %[tag] bitop3:0x7e\n\t" \
              "v_bitop3_b32 %[t1], v[" #b "+5], v[" #b "+7], %[tag] bitop3:0x7e\n\t" \
              "v_bitop3_b32 %[t2], v[" #b "+9], v[" #b "+11], %[tag] bitop3:0x7e\n\t" \
              "v_or3_b32 %[t0], %[t0], %[t1], %[t2]\n\t" \
              "v_bitop3_b32 %[t1], v[" #b "+13], v[" #b "+15], %[tag] bitop3:0x7e\n\t" \
              "v_or_b32 %[t0], %[t0], %[t1]\n\t" \
              "v_cmp_ne_u32 vcc, 0, %[t0]\n\t"
#define PM(b) "v_mov_b32 %[p0], v[" #b "]\n\tv_mov_b32 %[p1], v[" #b "+2]\n\tv_mov_b32 %[p2], v[" #b "+4]\n\tv_mov_b32 %[p3], v[" #b "+6]\n\t" \
              "v_mov_b32 %[p4], v[" #b "+8]\n\tv_mov_b32 %[p5], v[" #b "+10]\n\tv_mov_b32 %[p6], v[" #b "+12]\n\tv_mov_b32 %[p7], v[" #b "+14]\n\t"
  asm volatile(
      PL(136)
      "s_sleep %[dl]\n"
      "1:\n\t"
      PL(152)
      PC(136)
      "s_cbranch_vccz 2f\n\t"
      "s_sub_u32 %[cap], %[cap], 1\n\t"
      "s_cbranch_scc1 2f\n\t"
      PL(136)
      PC(152)
      "s_cbranch_vccnz 1b\n\t"
      PM(152)
      "s_branch 3f\n"
      "2:\n\t"
      PM(136)
      "3:\n"
      : [p0] "=&v"(p[0]), [p1] "=&v"(p[1]), [p2] "=&v"(p[2]), [p3] "=&v"(p[3]), [p4] "=&v"(p[4]), [p5] "=&v"(p[5]), [p6] "=&v"(p[6]), [p7] "=&v"(p[7]),
        [t0] "=&v"(t0), [t1] "=&v"(t1), [t2] "=&v"(t2), [cap] "+s"(cap)
      : [off] "v"(off), [rs] "s"(rs), [tag] "v"(tag), [s1] "s"(STRIDE), [s2] "s"(2 * STRIDE), [s3] "s"(3 * STRIDE), [dl] "i"(8)
      : "memory", "vcc", "scc",
        "v136", "v137", "v138", "v139", "v140", "v141", "v142", "v143", "v144", "v145", "v146", "v147", "v148", "v149", "v150", "v151",
        "v152", "v153", "v154", "v155", "v156", "v157", "v158", "v159", "v160", "v161", "v162", "v163", "v164", "v165", "v166", "v167");
#undef PL
#undef PC
#undef PM
}

}

extern "C" __global__ void __launch_bounds__(NT, (NW + NS) / 4)
fused_decode_kernel(const unsigned char* __restrict__ x, const unsigned char* __restrict__ kc,
                    const unsigned char* __restrict__ vc, const unsigned char* __restrict__ wq,
                    const unsigned char* __restrict__ wo, unsigned char* out, unsigned char* ws) {
  __shared__ __attribute__((aligned(16))) unsigned char smem_[L_BYTES];
  LAS unsigned char* smem = (LAS unsigned char*)smem_;
  const unsigned lds0 = (unsigned)(size_t)smem;
  const int tid = threadIdx.x, lane = tid & 63;
  const int wave = __builtin_amdgcn_readfirstlane(tid >> 6);
  const int bid = blockIdx.x, jj = bid >> 3, b = jj & 15, kvh = 2 * ((bid + (b >> 1)) & 7) + (jj >> 4);
  const int li = lane & 15, lg = lane >> 4;
  volatile LAS unsigned* flag = (volatile LAS unsigned*)(smem + L_FLAG);
#define PACE(n) do { __builtin_amdgcn_sched_barrier(0); asm volatile("s_waitcnt vmcnt(%0)" :: "i"((n) - 1) : "memory"); } while (0)

  if (wave >= NW) {
    const int t = wave - NW;
    const __amdgpu_buffer_rsrc_t rs_ws = __builtin_amdgcn_make_buffer_rsrc(ws, 0, WS_BYTES, RSRC_W3);
    if (t == 0) { flag[0] = 0u; flag[1] = 0u; flag[3] = 0u; flag[4] = 0u; flag[5] = 0u; }
    __builtin_amdgcn_s_setprio(3);
    if (t != 0) __syncthreads();
    if (t == 0) {
      if (b >= 8) {
        const __amdgpu_buffer_rsrc_t rs_x = __builtin_amdgcn_make_buffer_rsrc((void*)x, 0, NB * DM * 2, RSRC_W3);
        const unsigned vx = (unsigned)(lane >> 2) * (DM * 2) + (unsigned)(lane & 3) * 16;
#pragma unroll 1
        for (int s = 0; s < 18; ++s) dma16(lds0 + L_X + s * 1024, vx, rs_x, (unsigned)s * 64);
      }
      unsigned epoch = __builtin_amdgcn_raw_buffer_load_b32(rs_ws, WS_EPOCH, 0, AUX_SC1);
      __builtin_amdgcn_sched_barrier(0);
      __syncthreads();
      asm volatile("s_waitcnt vmcnt(0)" : "+v"(epoch) :: "memory");
      const unsigned tg = epoch + 1u;
      flag[2] = tg;
      if (b >= 8) {
        while (flag[3] != (unsigned)NW) __builtin_amdgcn_s_sleep(1);
        asm volatile("" ::: "memory");
        f32x4 a0 = {0.f, 0.f, 0.f, 0.f}, a1 = {0.f, 0.f, 0.f, 0.f};
        LAS unsigned char* pw = smem + L_WQ + (8 * lg + ((lane >> 2) & 3)) * 64 + (lane & 3) * 8;
        LAS unsigned char* px = smem + L_X + li * 64 + lg * 16;
#pragma unroll 6
        for (int s = 0; s < 18; ++s) {
          const u32x2 w0 = trread(pw + s * 2048), w1 = trread(pw + s * 2048 + 256);
          const u32x2 w2 = trread(pw + s * 2048 + 32), w3 = trread(pw + s * 2048 + 256 + 32);
          const u32x4 xa = *(LAS u32x4*)(px + s * 1024);
          a0 = mfma16(xa, u32x4{w0[0], w0[1], w1[0], w1[1]}, a0);
          a1 = mfma16(xa, u32x4{w2[0], w2[1], w3[0], w3[1]}, a1);
        }
        LAS unsigned* tq = (LAS unsigned*)(smem + L_OA);
#pragma unroll
        for (int r = 0; r < 4; ++r) tq[(4 * lg + r) * 16 + li] = pkbf(a0[r], a1[r]);
        asm volatile("s_waitcnt lgkmcnt(0)" ::: "memory");
        const unsigned qo = WS_QG + (unsigned)(kvh * 16 + (lane >> 3)) * 1024 + (unsigned)(b - 8) * 128 + (unsigned)(lane & 7) * 16;
#pragma unroll
        for (int k = 0; k < 2; ++k) {
          const u32x2 v = *(LAS u32x2*)(tq + k * 128 + lane * 2);
          __builtin_amdgcn_raw_buffer_store_b128(u32x4{v[0], tg, v[1], tg}, rs_ws, qo, k * 8192, AUX_SC1);
        }
        __builtin_amdgcn_s_sleep(32);
      } else {
        __builtin_amdgcn_s_sleep(96);
      }
      const unsigned qi = WS_QG + (unsigned)(kvh * 16 + b) * 1024 + (unsigned)lane * 16;
      u32x4 qr;
      for (unsigned spins = 0;; ++spins) {
        asm volatile("" ::: "memory");
        qr = __builtin_amdgcn_raw_buffer_load_b128(rs_ws, qi, 0, AUX_SC1);
        if (__builtin_amdgcn_ballot_w64(((qr[1] ^ tg) | (qr[3] ^ tg)) != 0u) == 0ull || spins > SPIN_CAP) break;
        __builtin_amdgcn_s_sleep(4);
      }
      {
        LAS unsigned* ql = (LAS unsigned*)(smem + L_Q) + 16 * (lane >> 3) + (lane & 7);
        ql[0] = __builtin_amdgcn_perm(qr[2], qr[0], 0x05040100u);
        ql[8] = __builtin_amdgcn_perm(qr[2], qr[0], 0x07060302u);
      }
      asm volatile("s_waitcnt lgkmcnt(0)" ::: "memory");
      flag[0] = 1u;
    }
    while (flag[0] == 0u) __builtin_amdgcn_s_sleep(16);
    asm volatile("" ::: "memory");
    const unsigned tag = flag[2];
    const __amdgpu_buffer_rsrc_t rs_out = __builtin_amdgcn_make_buffer_rsrc(out, 0, NB * DM * 2, RSRC_W3);
    u32x4 wf[8];
    LAS unsigned char* pwf = smem + ((lane >> 2) & 3) * 1024 + lg * 128 + ((t ^ (lane >> 2)) & 3) * 32 + (lane & 3) * 8;
#define LOADWF(dst, base) _Pragma("unroll") for (int s = 0; s < 8; ++s) { const int ro = ((s & 1) * 128 + 32 * (s >> 1)) * 128; \
        const u32x2 w0 = trread(pwf + (base) + ro), w1 = trread(pwf + (base) + ro + 512); dst[s] = u32x4{w0[0], w0[1], w1[0], w1[1]}; }
    const bool st1 = b < 8 || b == 13;
    const int cs = b < 8 ? b : 8;
    const int kv1 = 2 * (bid & 7) + (jj >> 4);
    const int rho = (b - 8) * 16 + kvh, T = rho >> 1, hf = rho & 1;
    unsigned o_pub = WS_OG + (unsigned)(kvh * 16 + b) * 1024 + (unsigned)lane * 16;
    unsigned o_get = st1 ? WS_OG + (unsigned)(kv1 * 16 + 4 * t) * 1024 + (unsigned)lane * 16
                         : WS_PG + (unsigned)((T * 16 + lg) * 2 + hf) * 1024 + (unsigned)(16 * t + li) * 16;
    unsigned o_put = st1 ? WS_PG + (unsigned)(((4 * cs + t) * 16 + kv1) * 2) * 1024 + (unsigned)lane * 16
                         : (lg == 0 ? (unsigned)(li * DM + 16 * T + 4 * t + 2 * hf) * 2 : OOB);
    asm volatile("" : "+v"(o_pub), "+v"(o_get), "+v"(o_put));
    if (st1 && t > 0) {
      const __amdgpu_buffer_rsrc_t rs_wo = __builtin_amdgcn_make_buffer_rsrc((void*)wo, 0, NQ * DM * 2, RSRC_W3);
      const unsigned vwo = wo_src(lane);
      while (flag[5] != (unsigned)NW) __builtin_amdgcn_s_sleep(2);
#pragma unroll 1
      for (int i = t - 1; i < 32; i += 3)
        dma16(lds0 + L_WO + i * 1024, vwo ^ (unsigned)((i & 3) * 32), rs_wo, (unsigned)(kv1 * 256 + 8 * i) * (DM * 2) + (unsigned)cs * 128);
    }
    __syncthreads();
    if (t == 0) {
      f32x4 pr[NW];
#pragma unroll
      for (int w = 0; w < NW; ++w) pr[w] = *(LAS f32x4*)(smem + L_RED + w * 1024 + lane * 16);
      __builtin_amdgcn_sched_barrier(0);
#pragma unroll
      for (int s = 1; s < NW; s *= 2)
#pragma unroll
        for (int w = 0; w < NW; w += 2 * s) pr[w] += pr[w + s];
      const f32x4 o = pr[0];
      __builtin_amdgcn_raw_buffer_store_b128(u32x4{pkbf(o[0], o[1]), tag, pkbf(o[2], o[3]), tag}, rs_ws, o_pub, 0, AUX_SC1);
    }
    unsigned p[8];
    if (st1) {
      if (t > 0) {
        asm volatile("s_waitcnt vmcnt(0)" ::: "memory");
        if (lane == 0) __hip_atomic_fetch_add((LAS unsigned*)(smem + L_FLAG + 16), 1u, __ATOMIC_RELAXED, __HIP_MEMORY_SCOPE_WORKGROUP);
      }
      while (flag[4] != 3u) __builtin_amdgcn_s_sleep(1);
      asm volatile("" ::: "memory");
      LOADWF(wf, L_WO)
      __builtin_amdgcn_sched_barrier(0);
      poll4<1024>(p, rs_ws, o_get, tag);
#pragma unroll
      for (int i = 0; i < 4; ++i) *(LAS u32x2*)(smem + L_OA + (4 * t + i) * 528 + lane * 8) = u32x2{p[2 * i], p[2 * i + 1]};
      __syncthreads();
      u32x4 ob[8];
#pragma unroll
      for (int s = 0; s < 8; ++s) ob[s] = *(LAS u32x4*)(smem + L_OA + li * 528 + s * 64 + lg * 16);
      __builtin_amdgcn_sched_barrier(0);
      f32x4 d = {0.f, 0.f, 0.f, 0.f};
#pragma unroll
      for (int s = 0; s < 8; ++s) d = mfma16(wf[s], ob[s], d);
      __builtin_amdgcn_raw_buffer_store_b128(u32x4{__float_as_uint(d[0]), tag, __float_as_uint(d[1]), tag}, rs_ws, o_put, 0, AUX_SC1);
      __builtin_amdgcn_raw_buffer_store_b128(u32x4{__float_as_uint(d[2]), tag, __float_as_uint(d[3]), tag}, rs_ws, o_put, 1024, AUX_SC1);
    } else if (rho < 72) {
      poll4<4 * 2048>(p, rs_ws, o_get, tag);
      float s0 = (__uint_as_float(p[0]) + __uint_as_float(p[2])) + (__uint_as_float(p[4]) + __uint_as_float(p[6]));
      float s1 = (__uint_as_float(p[1]) + __uint_as_float(p[3])) + (__uint_as_float(p[5]) + __uint_as_float(p[7]));
      s0 = xrow_sum(s0); s1 = xrow_sum(s1);
      __builtin_amdgcn_raw_buffer_store_b32(pkbf(s0, s1), rs_out, o_put, 0, 0);
      if (rho == 0 && t == 0) __builtin_amdgcn_raw_buffer_store_b32(tag, rs_ws, WS_EPOCH, 0, AUX_SC1);
    }
    asm volatile("s_waitcnt vmcnt(0)" ::: "memory");
    return;
  }

  const int sw = wave;
  const unsigned tb = (unsigned)__builtin_amdgcn_s_memrealtime();
  const bool isq = b >= 8;
  if (isq) {
    const __amdgpu_buffer_rsrc_t rs_w = __builtin_amdgcn_make_buffer_rsrc((void*)wq, 0, DM * NQ * 2, RSRC_W3);
    const unsigned vw = (unsigned)(lane >> 2) * (NQ * 2) + (unsigned)(lane & 3) * 16 + (unsigned)(kvh * 256 + 32 * (b - 8)) * 2;
    int cnt = 0;
#pragma unroll 1
    for (int i = sw; i < 36; i += NW) {
      if (cnt >= KWIN) PACE(KWIN);
      dma16(lds0 + L_WQ + i * 1024, vw, rs_w, (unsigned)i * (16 * NQ * 2));
      if (++cnt == KWIN) __syncthreads();
    }
    static_assert(KWIN <= 4, "every streaming wave must reach the barrier inside the weight loop");
  }
  const size_t kvoff = ((size_t)(b * HKV + kvh) * SK + (SK - WIN)) * (DH * 2);
  const __amdgpu_buffer_rsrc_t rs_k = __builtin_amdgcn_make_buffer_rsrc((void*)(kc + kvoff), 0, WIN * DH * 2, RSRC_W3);
  const __amdgpu_buffer_rsrc_t rs_v = __builtin_amdgcn_make_buffer_rsrc((void*)(vc + kvoff), 0, WIN * DH * 2, RSRC_W3);
  const unsigned voff = (unsigned)lane * 16 + (unsigned)sw * (NL * 1024);
  const unsigned ph = (unsigned)sw * 4096;
#define SOFF(u) (((u) * 1024 + ph) & (NL * 1024 - 1))
  u32x4 K[NL], V[NL], qA, qB;
  float y[NL], m = -INFINITY, mc = 0.f, l = 0.f, f = 0.f;
  const int hA = li >> 3;
  constexpr float C2 = 0.088388347648318440f * 1.4426950408889634f;
  const unsigned hm = (lane & 1) ? 0xffff0000u : 0x0000ffffu;
  const int rsel = (lane & 7) >> 1;
  const unsigned mk0 = rsel == 0 ? hm : 0u, mk1 = rsel == 1 ? hm : 0u, mk2 = rsel == 2 ? hm : 0u, mk3 = rsel == 3 ? hm : 0u;
  f32x4 acc = {0.f, 0.f, 0.f, 0.f};
  unsigned tnow = tb;
#define PV(u) do { const unsigned pp = pkbf(y[u], y[u]); acc = mfma16(V[u], u32x4{pp & mk0, pp & mk1, pp & mk2, pp & mk3}, acc); } while (0)
  sfor<2 * NL>([&](auto ic) __attribute__((always_inline)) {
    constexpr int i = decltype(ic)::value;
    if constexpr (i < KWIN) PACE(KWIN);
    else {
      PACE(VWIN);
      if (tnow - tb < (unsigned)((i - 1 + TOFF) * SLOT_NS / 10)) PACE(KWIN);
      __builtin_amdgcn_sched_barrier(0);
    }
    if constexpr (i == KWIN - 1) {
      if (isq && lane == 0) __hip_atomic_fetch_add((LAS unsigned*)(smem + L_FLAG + 12), 1u, __ATOMIC_RELAXED, __HIP_MEMORY_SCOPE_WORKGROUP);
    }
    if constexpr (i < NL) K[i] = __builtin_amdgcn_raw_buffer_load_b128(rs_k, voff, SOFF(i), AUX_NT);
    else V[i - NL] = __builtin_amdgcn_raw_buffer_load_b128(rs_v, voff, SOFF(i - NL), AUX_NT);
    __builtin_amdgcn_sched_barrier(0);
    tnow = (unsigned)__builtin_amdgcn_s_memrealtime();
    __builtin_amdgcn_sched_barrier(0);
    if constexpr (i == KWIN - 1) {
      if (!isq) __syncthreads();
    }
    if constexpr (i == 2 * NL - 1) {
      if (lane == 0) __hip_atomic_fetch_add((LAS unsigned*)(smem + L_FLAG + 20), 1u, __ATOMIC_RELAXED, __HIP_MEMORY_SCOPE_WORKGROUP);
      __builtin_amdgcn_sched_barrier(0);
    }
    if constexpr (i == QS) {
      while (flag[0] == 0u) __builtin_amdgcn_s_sleep(1);
      asm volatile("" ::: "memory");
      qA = *(LAS u32x4*)(smem + L_Q + hA * 256 + li * 16);
      qB = *(LAS u32x4*)(smem + L_Q + (hA ^ 1) * 256 + li * 16);
    }
    if constexpr (i >= QS && i < ES) {
#pragma unroll
      for (int u = 2 * (i - QS); u < 2 * (i - QS) + 2; ++u) {
        float xa = 0.f, xb = 0.f;
#pragma unroll
        for (int k = 0; k < 4; ++k) { xa = dot2(K[u][k], qA[k], xa); xb = dot2(K[u][k], qB[k], xb); }
        float t = xa + dpp<ROR8>(xb);
        t += dpp<XOR1>(t);
        t += dpp<XOR2>(t);
        t += dpp<HMIRROR>(t);
        y[u] = t;
        m = fmaxf(m, t);
        asm volatile("" : "+v"(y[u]), "+v"(m));
      }
    }
    if constexpr (i == ES) mc = xrow_max(m) * C2;
    if constexpr (i >= ES && i < PS) {
#pragma unroll
      for (int u = (NL / 4) * (i - ES); u < (NL / 4) * (i - ES + 1); ++u) { y[u] = __builtin_amdgcn_exp2f(fmaf(y[u], C2, -mc)); l += y[u]; }
      asm volatile("" : "+v"(l));
    }
    if constexpr (i == PS - 1) {
      l = xrow_sum(l);
      *(LAS f32x2*)(smem + L_ML + hA * (NW * 8) + sw * 8) = f32x2{mc, l};
      asm volatile("s_waitcnt lgkmcnt(0)" ::: "memory");
      if (lane == 0) __hip_atomic_fetch_add((LAS unsigned*)(smem + L_FLAG + 4), 1u, __ATOMIC_RELAXED, __HIP_MEMORY_SCOPE_WORKGROUP);
    }
    if constexpr (i >= PS) {
#pragma unroll
      for (int u = npv_after(i - 1); u < npv_after(i); ++u) PV(u);
      asm volatile("" : "+v"(acc));
    }
    if constexpr (i == FIN_AT) {
      __builtin_amdgcn_sched_barrier(0);
      while (flag[1] != (unsigned)NW) __builtin_amdgcn_s_sleep(1);
      asm volatile("" ::: "memory");
      f32x4 ml[NW / 2];
#pragma unroll
      for (int w = 0; w < NW / 2; ++w) ml[w] = *(LAS f32x4*)(smem + L_ML + hA * (NW * 8) + w * 16);
      float M = fmaxf(ml[0][0], ml[0][2]);
#pragma unroll
      for (int w = 1; w < NW / 2; ++w) M = fmaxf(M, fmaxf(ml[w][0], ml[w][2]));
      float L = 0.f;
#pragma unroll
      for (int w = 0; w < NW / 2; ++w) {
        L = fmaf(ml[w][1], __builtin_amdgcn_exp2f(ml[w][0] - M), L);
        L = fmaf(ml[w][3], __builtin_amdgcn_exp2f(ml[w][2] - M), L);
      }
      f = __builtin_amdgcn_exp2f(mc - M) / L;
      asm volatile("" : "+v"(f));
    }
  });
  __builtin_amdgcn_sched_barrier(0);
#pragma unroll
  for (int u = npv_after(2 * NL - 1); u < NL; ++u) PV(u);
  *(LAS f32x4*)(smem + L_RED + sw * 1024 + lane * 16) = acc * f;
  __syncthreads();
}

extern "C" void kernel_launch(void* const* d_in, const int* in_sizes, int n_in, void* d_out, int out_size,
                              void* d_ws, size_t ws_size, hipStream_t stream) {
  fused_decode_kernel<<<256, NT, 0, stream>>>((const unsigned char*)d_in[0], (const unsigned char*)d_in[1],
                                             (const unsigned char*)d_in[2], (const unsigned char*)d_in[3],
                                             (const unsigned char*)d_in[4], (unsigned char*)d_out,
                                             (unsigned char*)d_ws);
}
```

```cpp
#define DQ_ 100
#include <hip/hip_runtime.h>
#include <hip/hip_bf16.h>
#include <stdint.h>
#include <utility>

#define LAS __attribute__((address_space(3)))
typedef unsigned u32x4 __attribute__((ext_vector_type(4)));
typedef unsigned u32x2 __attribute__((ext_vector_type(2)));
typedef float f32x4 __attribute__((ext_vector_type(4)));
typedef float f32x2 __attribute__((ext_vector_type(2)));
typedef short s16x4 __attribute__((ext_vector_type(4)));
typedef __bf16 bf16x8 __attribute__((ext_vector_type(8)));
typedef __bf16 bf16x2 __attribute__((ext_vector_type(2)));

#define KW_ 3
#define VW_ 5
#define SL_ 294
#define TO_ 5
#define WQP_ 0
#define WOP_ 0
#define MAP_ 1
#define G2_ 0
#define G2D_ 0
#define W13_ 63
namespace {

constexpr int NB = 16, DM = 576, HKV = 16, SK = 4096, DH = 128, WIN = 1024, NQ = 4096;
constexpr int NW = 8, NS = 4, NT = (NW + NS) * 64;
constexpr int NL = 256 / NW;

constexpr int KWIN = KW_, VWIN = VW_;
constexpr int SLOT_NS = SL_, TOFF = TO_;
constexpr int QS = 26;
constexpr int FIN_AT = 58;
constexpr int AUX_NT = 2, AUX_SC1 = 16;
constexpr int RSRC_W3 = 0x00020000;
constexpr unsigned OOB = 0x7ffffff0u;

constexpr unsigned WS_EPOCH = 0;
constexpr unsigned WS_QG = 4096;
constexpr unsigned WS_OG = WS_QG + 256 * 1024;
constexpr unsigned WS_PG = WS_OG + 256 * 1024;
constexpr unsigned WS_BYTES = WS_PG + 36 * 16 * 2 * 1024;

constexpr int L_WQ = 0;
constexpr int L_X = L_WQ + 36864;
constexpr int L_WO = L_X + 18432;
constexpr int L_RED = L_WO + 32768;
constexpr int L_OA = L_RED + NW * 1024;
constexpr int L_Q = L_OA + 16 * 528;
constexpr int L_ML = L_Q + 512;
constexpr int L_FLAG = L_ML + NW * 16;
constexpr int L_BYTES = L_FLAG + 32;

constexpr unsigned SPIN_CAP = 1u << 18;

template <class F, int... I>
__device__ __forceinline__ void sfor_impl(F&& f, std::integer_sequence<int, I...>) { (f(std::integral_constant<int, I>{}), ...); }
template <int N, class F>
__device__ __forceinline__ void sfor(F&& f) { sfor_impl(f, std::make_integer_sequence<int, N>{}); }
constexpr int ES = QS + NL / 2, PS = ES + 4;
constexpr int npv_after(int slot) {
  int n = 0;
  for (int i = PS; i <= slot; ++i)
    for (int k = 0; k < 2; ++k)
      if (n <= i - NL - VWIN) ++n;
  return n;
}

__device__ __forceinline__ float dot2(unsigned k, unsigned q, float acc) {
  return __builtin_amdgcn_fdot2_f32_bf16(__builtin_bit_cast(bf16x2, k), __builtin_bit_cast(bf16x2, q), acc, false);
}
__device__ __forceinline__ unsigned pkbf(float lo, float hi) {
  f32x2 f = {lo, hi};
  return __builtin_bit_cast(unsigned, __builtin_convertvector(f, bf16x2));
}
template <int CTRL> __device__ __forceinline__ float dpp(float x) {
  return __builtin_bit_cast(float, __builtin_amdgcn_mov_dpp(__builtin_bit_cast(int, x), CTRL, 0xf, 0xf, true));
}
constexpr int XOR1 = 0xB1, XOR2 = 0x4E, HMIRROR = 0x141, ROR8 = 0x128;
__device__ __forceinline__ float xrow_max(float x) {
  auto s = __builtin_amdgcn_permlane16_swap(__float_as_uint(x), __float_as_uint(x), false, false);
  x = fmaxf(__uint_as_float(s[0]), __uint_as_float(s[1]));
  auto t = __builtin_amdgcn_permlane32_swap(__float_as_uint(x), __float_as_uint(x), false, false);
  return fmaxf(__uint_as_float(t[0]), __uint_as_float(t[1]));
}
__device__ __forceinline__ float xrow_sum(float x) {
  auto s = __builtin_amdgcn_permlane16_swap(__float_as_uint(x), __float_as_uint(x), false, false);
  x = __uint_as_float(s[0]) + __uint_as_float(s[1]);
  auto t = __builtin_amdgcn_permlane32_swap(__float_as_uint(x), __float_as_uint(x), false, false);
  return __uint_as_float(t[0]) + __uint_as_float(t[1]);
}
__device__ __forceinline__ f32x4 mfma16(u32x4 a, u32x4 b, f32x4 c) {
  return __builtin_amdgcn_mfma_f32_16x16x32_bf16(__builtin_bit_cast(bf16x8, a), __builtin_bit_cast(bf16x8, b), c, 0, 0, 0);
}
template <int NTP>
__device__ __forceinline__ void dma16(unsigned lds, unsigned voff, __amdgpu_buffer_rsrc_t rs, unsigned soff) {
  if constexpr (NTP == 1)
    asm volatile("s_mov_b32 m0, %0\n\ts_nop 0\n\tbuffer_load_dwordx4 %1, %2, %3 offen nt lds"
                 :: "s"(lds), "v"(voff), "s"(rs), "s"(soff) : "memory");
  else
    asm volatile("s_mov_b32 m0, %0\n\ts_nop 0\n\tbuffer_load_dwordx4 %1, %2, %3 offen lds"
                 :: "s"(lds), "v"(voff), "s"(rs), "s"(soff) : "memory");
}
__device__ __forceinline__ u32x2 trread(LAS unsigned char* p) {
  s16x4 v = __builtin_amdgcn_ds_read_tr16_b64_v4i16((LAS s16x4*)p);
  return __builtin_bit_cast(u32x2, v);
}
__device__ __forceinline__ unsigned wo_src(int lane) {
  const int row = ((lane >> 4) & 1) * 4 + ((lane >> 3) & 1) * 2 + (lane >> 5);
  return (unsigned)row * (DM * 2) + (unsigned)(lane & 7) * 16;
}
template <int STRIDE>
__device__ __forceinline__ void poll4(unsigned (&p)[8], __amdgpu_buffer_rsrc_t rs, unsigned off, unsigned tag) {
  unsigned cap = SPIN_CAP, t0, t1, t2;
#define PL(b) "buffer_load_dwordx4 v[" #b ":" #b "+3], %[off], %[rs], 0 offen sc1\n\t" \
              "buffer_load_dwordx4 v[" #b "+4:" #b "+7], %[off], %[rs], %[s1] offen sc1\n\t" \
              "buffer_load_dwordx4 v[" #b "+8:" #b "+11], %[off], %[rs], %[s2] offen sc1\n\t" \
              "buffer_load_dwordx4 v[" #b "+12:" #b "+15], %[off], %[rs], %[s3] offen sc1\n\t"
#define PC(b) "s_waitcnt vmcnt(4)\n\t" \
              "v_bitop3_b32 %[t0], v[" #b "+1], v[" #b "+3], %[tag] bitop3:0x7e\n\t" \
              "v_bitop3_b32 %[t1], v[" #b "+5], v[" #b "+7], %[tag] bitop3:0x7e\n\t" \
              "v_bitop3_b32 %[t2], v[" #b "+9], v[" #b "+11], %[tag] bitop3:0x7e\n\t" \
              "v_or3_b32 %[t0], %[t0], %[t1], %[t2]\n\t" \
              "v_bitop3_b32 %[t1], v[" #b "+13], v[" #b "+15], %[tag] bitop3:0x7e\n\t" \
              "v_or_b32 %[t0], %[t0], %[t1]\n\t" \
              "v_cmp_ne_u32 vcc, 0, %[t0]\n\t"
#define PM(b) "v_mov_b32 %[p0], v[" #b "]\n\tv_mov_b32 %[p1], v[" #b "+2]\n\tv_mov_b32 %[p2], v[" #b "+4]\n\tv_mov_b32 %[p3], v[" #b "+6]\n\t" \
              "v_mov_b32 %[p4], v[" #b "+8]\n\tv_mov_b32 %[p5], v[" #b "+10]\n\tv_mov_b32 %[p6], v[" #b "+12]\n\tv_mov_b32 %[p7], v[" #b "+14]\n\t"
  asm volatile(
      PL(136)
      "s_sleep %[dl]\n"
      "1:\n\t"
      PL(152)
      PC(136)
      "s_cbranch_vccz 2f\n\t"
      "s_sub_u32 %[cap], %[cap], 1\n\t"
      "s_cbranch_scc1 2f\n\t"
      PL(136)
      PC(152)
      "s_cbranch_vccnz 1b\n\t"
      PM(152)
      "s_branch 3f\n"
      "2:\n\t"
      PM(136)
      "3:\n"
      : [p0] "=&v"(p[0]), [p1] "=&v"(p[1]), [p2] "=&v"(p[2]), [p3] "=&v"(p[3]), [p4] "=&v"(p[4]), [p5] "=&v"(p[5]), [p6] "=&v"(p[6]), [p7] "=&v"(p[7]),
        [t0] "=&v"(t0), [t1] "=&v"(t1), [t2] "=&v"(t2), [cap] "+s"(cap)
      : [off] "v"(off), [rs] "s"(rs), [tag] "v"(tag), [s1] "s"(STRIDE), [s2] "s"(2 * STRIDE), [s3] "s"(3 * STRIDE), [dl] "i"(8)
      : "memory", "vcc", "scc",
        "v136", "v137", "v138", "v139", "v140", "v141", "v142", "v143", "v144", "v145", "v146", "v147", "v148", "v149", "v150", "v151",
        "v152", "v153", "v154", "v155", "v156", "v157", "v158", "v159", "v160", "v161", "v162", "v163", "v164", "v165", "v166", "v167");
#undef PL
#undef PC
#undef PM
}

}

extern "C" __global__ void __launch_bounds__(NT, (NW + NS) / 4)
fused_decode_kernel(const unsigned char* __restrict__ x, const unsigned char* __restrict__ kc,
                    const unsigned char* __restrict__ vc, const unsigned char* __restrict__ wq,
                    const unsigned char* __restrict__ wo, unsigned char* out, unsigned char* ws) {
  __shared__ __attribute__((aligned(16))) unsigned char smem_[L_BYTES];
  LAS unsigned char* smem = (LAS unsigned char*)smem_;
  const unsigned lds0 = (unsigned)(size_t)smem;
  const int tid = threadIdx.x, lane = tid & 63;
  const int wave = __builtin_amdgcn_readfirstlane(tid >> 6);
  const int bid = blockIdx.x, jj = bid >> 3, b = jj & 15, kvh = 2 * ((bid + (MAP_ ? (b >> 1) : 0)) & 7) + (jj >> 4);
  const int li = lane & 15, lg = lane >> 4;
  volatile LAS unsigned* flag = (volatile LAS unsigned*)(smem + L_FLAG);
#define PACE(n) do { __builtin_amdgcn_sched_barrier(0); asm volatile("s_waitcnt vmcnt(%0)" :: "i"((n) - 1) : "memory"); } while (0)

  if (wave >= NW) {
    const int t = wave - NW;
    const __amdgpu_buffer_rsrc_t rs_ws = __builtin_amdgcn_make_buffer_rsrc(ws, 0, WS_BYTES, RSRC_W3);
    if (t == 0) { flag[0] = 0u; flag[1] = 0u; flag[3] = 0u; flag[4] = 0u; flag[5] = 0u; }
    __builtin_amdgcn_s_setprio(3);
    if (t != 0) __syncthreads();
    if (t == 0) {
      if (b >= 8) {
        const __amdgpu_buffer_rsrc_t rs_x = __builtin_amdgcn_make_buffer_rsrc((void*)x, 0, NB * DM * 2, RSRC_W3);
        const unsigned vx = (unsigned)(lane >> 2) * (DM * 2) + (unsigned)(lane & 3) * 16;
#pragma unroll 1
        for (int s = 0; s < 18; ++s) dma16<0>(lds0 + L_X + s * 1024, vx, rs_x, (unsigned)s * 64);
      }
      unsigned epoch = __builtin_amdgcn_raw_buffer_load_b32(rs_ws, WS_EPOCH, 0, AUX_SC1);
      __builtin_amdgcn_sched_barrier(0);
      __syncthreads();
      asm volatile("s_waitcnt vmcnt(0)" : "+v"(epoch) :: "memory");
      const unsigned tg = epoch + 1u;
      flag[2] = tg;
      if (b >= 8) {
        while (flag[3] != (unsigned)NW) __builtin_amdgcn_s_sleep(1);
        asm volatile("" ::: "memory");
        f32x4 a0 = {0.f, 0.f, 0.f, 0.f}, a1 = {0.f, 0.f, 0.f, 0.f};
        LAS unsigned char* pw = smem + L_WQ + (8 * lg + ((lane >> 2) & 3)) * 64 + (lane & 3) * 8;
        LAS unsigned char* px = smem + L_X + li * 64 + lg * 16;
#pragma unroll 6
        for (int s = 0; s < 18; ++s) {
          const u32x2 w0 = trread(pw + s * 2048), w1 = trread(pw + s * 2048 + 256);
          const u32x2 w2 = trread(pw + s * 2048 + 32), w3 = trread(pw + s * 2048 + 256 + 32);
          const u32x4 xa = *(LAS u32x4*)(px + s * 1024);
          a0 = mfma16(xa, u32x4{w0[0], w0[1], w1[0], w1[1]}, a0);
          a1 = mfma16(xa, u32x4{w2[0], w2[1], w3[0], w3[1]}, a1);
        }
        LAS unsigned* tq = (LAS unsigned*)(smem + L_OA);
#pragma unroll
        for (int r = 0; r < 4; ++r) tq[(4 * lg + r) * 16 + li] = pkbf(a0[r], a1[r]);
        asm volatile("s_waitcnt lgkmcnt(0)" ::: "memory");
        const unsigned qo = WS_QG + (unsigned)(kvh * 16 + (lane >> 3)) * 1024 + (unsigned)(b - 8) * 128 + (unsigned)(lane & 7) * 16;
#pragma unroll
        for (int k = 0; k < 2; ++k) {
          const u32x2 v = *(LAS u32x2*)(tq + k * 128 + lane * 2);
          __builtin_amdgcn_raw_buffer_store_b128(u32x4{v[0], tg, v[1], tg}, rs_ws, qo, k * 8192, AUX_SC1);
        }
        __builtin_amdgcn_s_sleep(32);
      } else {
        __builtin_amdgcn_s_sleep(96);
      }
      const unsigned qi = WS_QG + (unsigned)(kvh * 16 + b) * 1024 + (unsigned)lane * 16;
      u32x4 qr;
      for (unsigned spins = 0;; ++spins) {
        asm volatile("" ::: "memory");
        qr = __builtin_amdgcn_raw_buffer_load_b128(rs_ws, qi, 0, AUX_SC1);
        if (__builtin_amdgcn_ballot_w64(((qr[1] ^ tg) | (qr[3] ^ tg)) != 0u) == 0ull || spins > SPIN_CAP) break;
        __builtin_amdgcn_s_sleep(4);
      }
      {
        LAS unsigned* ql = (LAS unsigned*)(smem + L_Q) + 16 * (lane >> 3) + (lane & 7);
        ql[0] = __builtin_amdgcn_perm(qr[2], qr[0], 0x05040100u);
        ql[8] = __builtin_amdgcn_perm(qr[2], qr[0], 0x07060302u);
      }
      asm volatile("s_waitcnt lgkmcnt(0)" ::: "memory");
      flag[0] = 1u;
    }
    while (flag[0] == 0u) __builtin_amdgcn_s_sleep(16);
    asm volatile("" ::: "memory");
    const unsigned tag = flag[2];
    const __amdgpu_buffer_rsrc_t rs_out = __builtin_amdgcn_make_buffer_rsrc(out, 0, NB * DM * 2, RSRC_W3);
    u32x4 wf[8];
    LAS unsigned char* pwf = smem + ((lane >> 2) & 3) * 1024 + lg * 128 + ((t ^ (lane >> 2)) & 3) * 32 + (lane & 3) * 8;
#define LOADWF(dst, base) _Pragma("unroll") for (int s = 0; s < 8; ++s) { const int ro = ((s & 1) * 128 + 32 * (s >> 1)) * 128; \
        const u32x2 w0 = trread(pwf + (base) + ro), w1 = trread(pwf + (base) + ro + 512); dst[s] = u32x4{w0[0], w0[1], w1[0], w1[1]}; }
    const bool st1 = b < 8 || b == 13;
    const int cs = b < 8 ? b : 8;
    const int kv1 = 2 * (bid & 7) + (jj >> 4);
    const int rho = (b - 8) * 16 + kvh, T = rho >> 1, hf = rho & 1;
    unsigned o_pub = WS_OG + (unsigned)(kvh * 16 + b) * 1024 + (unsigned)lane * 16;
    unsigned o_get = st1 ? WS_OG + (unsigned)(kv1 * 16 + 4 * t) * 1024 + (unsigned)lane * 16
                         : WS_PG + (unsigned)((T * 16 + lg) * 2 + hf) * 1024 + (unsigned)(16 * t + li) * 16;
    unsigned o_put = st1 ? WS_PG + (unsigned)(((4 * cs + t) * 16 + kv1) * 2) * 1024 + (unsigned)lane * 16
                         : (lg == 0 ? (unsigned)(li * DM + 16 * T + 4 * t + 2 * hf) * 2 : OOB);
    asm volatile("" : "+v"(o_pub), "+v"(o_get), "+v"(o_put));
    if (st1 && t > 0) {
      const __amdgpu_buffer_rsrc_t rs_wo = __builtin_amdgcn_make_buffer_rsrc((void*)wo, 0, NQ * DM * 2, RSRC_W3);
      const unsigned vwo = wo_src(lane);
      while (flag[5] != (unsigned)NW) __builtin_amdgcn_s_sleep(2);
#pragma unroll 1
      for (int i = t - 1; i < 32; i += 3)
        dma16<WOP_>(lds0 + L_WO + i * 1024, vwo ^ (unsigned)((i & 3) * 32), rs_wo, (unsigned)(kv1 * 256 + 8 * i) * (DM * 2) + (unsigned)cs * 128);
    }
    __syncthreads();
    if (t == 0) {
      f32x4 pr[NW];
#pragma unroll
      for (int w = 0; w < NW; ++w) pr[w] = *(LAS f32x4*)(smem + L_RED + w * 1024 + lane * 16);
      __builtin_amdgcn_sched_barrier(0);
#pragma unroll
      for (int s = 1; s < NW; s *= 2)
#pragma unroll
        for (int w = 0; w < NW; w += 2 * s) pr[w] += pr[w + s];
      const f32x4 o = pr[0];
      __builtin_amdgcn_raw_buffer_store_b128(u32x4{pkbf(o[0], o[1]), tag, pkbf(o[2], o[3]), tag}, rs_ws, o_pub, 0, AUX_SC1);
    }
    unsigned p[8];
    if (st1) {
      if (t > 0) {
        asm volatile("s_waitcnt vmcnt(0)" ::: "memory");
        if (lane == 0) __hip_atomic_fetch_add((LAS unsigned*)(smem + L_FLAG + 16), 1u, __ATOMIC_RELAXED, __HIP_MEMORY_SCOPE_WORKGROUP);
      }
      while (flag[4] != 3u) __builtin_amdgcn_s_sleep(1);
      asm volatile("" ::: "memory");
      LOADWF(wf, L_WO)
      __builtin_amdgcn_sched_barrier(0);
      poll4<1024>(p, rs_ws, o_get, tag);
#pragma unroll
      for (int i = 0; i < 4; ++i) *(LAS u32x2*)(smem + L_OA + (4 * t + i) * 528 + lane * 8) = u32x2{p[2 * i], p[2 * i + 1]};
      __syncthreads();
      u32x4 ob[8];
#pragma unroll
      for (int s = 0; s < 8; ++s) ob[s] = *(LAS u32x4*)(smem + L_OA + li * 528 + s * 64 + lg * 16);
      __builtin_amdgcn_sched_barrier(0);
      f32x4 d = {0.f, 0.f, 0.f, 0.f};
#pragma unroll
      for (int s = 0; s < 8; ++s) d = mfma16(wf[s], ob[s], d);
      __builtin_amdgcn_raw_buffer_store_b128(u32x4{__float_as_uint(d[0]), tag, __float_as_uint(d[1]), tag}, rs_ws, o_put, 0, AUX_SC1);
      __builtin_amdgcn_raw_buffer_store_b128(u32x4{__float_as_uint(d[2]), tag, __float_as_uint(d[3]), tag}, rs_ws, o_put, 1024, AUX_SC1);
    } else if (rho < 72) {
      poll4<4 * 2048>(p, rs_ws, o_get, tag);
      float s0 = (__uint_as_float(p[0]) + __uint_as_float(p[2])) + (__uint_as_float(p[4]) + __uint_as_float(p[6]));
      float s1 = (__uint_as_float(p[1]) + __uint_as_float(p[3])) + (__uint_as_float(p[5]) + __uint_as_float(p[7]));
      s0 = xrow_sum(s0); s1 = xrow_sum(s1);
      __builtin_amdgcn_raw_buffer_store_b32(pkbf(s0, s1), rs_out, o_put, 0, 0);
      if (rho == 0 && t == 0) __builtin_amdgcn_raw_buffer_store_b32(tag, rs_ws, WS_EPOCH, 0, AUX_SC1);
    }
    asm volatile("s_waitcnt vmcnt(0)" ::: "memory");
    return;
  }

  const int sw = wave;
  const bool isq = b >= 8;
  const unsigned tb = (unsigned)__builtin_amdgcn_s_memrealtime() - (isq ? (unsigned)(DQ_) : 0u);
  if (isq) {
    const __amdgpu_buffer_rsrc_t rs_w = __builtin_amdgcn_make_buffer_rsrc((void*)wq, 0, DM * NQ * 2, RSRC_W3);
    const unsigned vw = (unsigned)(lane >> 2) * (NQ * 2) + (unsigned)(lane & 3) * 16 + (unsigned)(kvh * 256 + 32 * (b - 8)) * 2;
    int cnt = 0;
#pragma unroll 1
    for (int i = sw; i < 36; i += NW) {
      if (cnt >= KWIN) PACE(KWIN);
      dma16<WQP_>(lds0 + L_WQ + i * 1024, vw, rs_w, (unsigned)i * (16 * NQ * 2));
      if (++cnt == KWIN) __syncthreads();
    }
    static_assert(KWIN <= 4, "every streaming wave must reach the barrier inside the weight loop");
  }
  const size_t kvoff = ((size_t)(b * HKV + kvh) * SK + (SK - WIN)) * (DH * 2);
  const __amdgpu_buffer_rsrc_t rs_k = __builtin_amdgcn_make_buffer_rsrc((void*)(kc + kvoff), 0, WIN * DH * 2, RSRC_W3);
  const __amdgpu_buffer_rsrc_t rs_v = __builtin_amdgcn_make_buffer_rsrc((void*)(vc + kvoff), 0, WIN * DH * 2, RSRC_W3);
  const unsigned voff = (unsigned)lane * 16 + (unsigned)sw * (NL * 1024);
  const unsigned ph = (unsigned)sw * 4096;
#define SOFF(u) (((u) * 1024 + ph) & (NL * 1024 - 1))
  u32x4 K[NL], V[NL], qA, qB;
  float y[NL], m = -INFINITY, mc = 0.f, l = 0.f, f = 0.f;
  const int hA = li >> 3;
  constexpr float C2 = 0.088388347648318440f * 1.4426950408889634f;
  const unsigned hm = (lane & 1) ? 0xffff0000u : 0x0000ffffu;
  const int rsel = (lane & 7) >> 1;
  const unsigned mk0 = rsel == 0 ? hm : 0u, mk1 = rsel == 1 ? hm : 0u, mk2 = rsel == 2 ? hm : 0u, mk3 = rsel == 3 ? hm : 0u;
  f32x4 acc = {0.f, 0.f, 0.f, 0.f};
  unsigned tnow = tb;
#define PV(u) do { const unsigned pp = pkbf(y[u], y[u]); acc = mfma16(V[u], u32x4{pp & mk0, pp & mk1, pp & mk2, pp & mk3}, acc); } while (0)
  sfor<2 * NL>([&](auto ic) __attribute__((always_inline)) {
    constexpr int i = decltype(ic)::value;
    if constexpr (i < KWIN) PACE(KWIN);
    else {
      PACE(VWIN);
      if (tnow - tb < (unsigned)((i - 1 + TOFF) * SLOT_NS / 10)) PACE(KWIN);
      __builtin_amdgcn_sched_barrier(0);
    }
    if constexpr (i == KWIN - 1) {
      if (isq && lane == 0) __hip_atomic_fetch_add((LAS unsigned*)(smem + L_FLAG + 12), 1u, __ATOMIC_RELAXED, __HIP_MEMORY_SCOPE_WORKGROUP);
    }
    if constexpr (i < NL) K[i] = __builtin_amdgcn_raw_buffer_load_b128(rs_k, voff, SOFF(i), AUX_NT);
    else V[i - NL] = __builtin_amdgcn_raw_buffer_load_b128(rs_v, voff, SOFF(i - NL), AUX_NT);
    __builtin_amdgcn_sched_barrier(0);
    tnow = (unsigned)__builtin_amdgcn_s_memrealtime();
    __builtin_amdgcn_sched_barrier(0);
    if constexpr (i == KWIN - 1) {
      if (!isq) __syncthreads();
    }
    if constexpr (i == W13_) {
      if (isq && lane == 0) __hip_atomic_fetch_add((LAS unsigned*)(smem + L_FLAG + 20), 1u, __ATOMIC_RELAXED, __HIP_MEMORY_SCOPE_WORKGROUP);
      __builtin_amdgcn_sched_barrier(0);
    }
    if constexpr (i == 2 * NL - 1) {
      if (!isq && lane == 0) __hip_atomic_fetch_add((LAS unsigned*)(smem + L_FLAG + 20), 1u, __ATOMIC_RELAXED, __HIP_MEMORY_SCOPE_WORKGROUP);
      __builtin_amdgcn_sched_barrier(0);
    }
    if constexpr (i == QS) {
      while (flag[0] == 0u) __builtin_amdgcn_s_sleep(1);
      asm volatile("" ::: "memory");
      qA = *(LAS u32x4*)(smem + L_Q + hA * 256 + li * 16);
      qB = *(LAS u32x4*)(smem + L_Q + (hA ^ 1) * 256 + li * 16);
    }
    if constexpr (i >= QS && i < ES) {
#pragma unroll
      for (int u = 2 * (i - QS); u < 2 * (i - QS) + 2; ++u) {
        float xa = 0.f, xb = 0.f;
#pragma unroll
        for (int k = 0; k < 4; ++k) { xa = dot2(K[u][k], qA[k], xa); xb = dot2(K[u][k], qB[k], xb); }
        float t = xa + dpp<ROR8>(xb);
        t += dpp<XOR1>(t);
        t += dpp<XOR2>(t);
        t += dpp<HMIRROR>(t);
        y[u] = t;
        m = fmaxf(m, t);
        asm volatile("" : "+v"(y[u]), "+v"(m));
      }
    }
    if constexpr (i == ES) mc = xrow_max(m) * C2;
    if constexpr (i >= ES && i < PS) {
#pragma unroll
      for (int u = (NL / 4) * (i - ES); u < (NL / 4) * (i - ES + 1); ++u) { y[u] = __builtin_amdgcn_exp2f(fmaf(y[u], C2, -mc)); l += y[u]; }
      asm volatile("" : "+v"(l));
    }
    if constexpr (i == PS - 1) {
      l = xrow_sum(l);
      *(LAS f32x2*)(smem + L_ML + hA * (NW * 8) + sw * 8) = f32x2{mc, l};
      asm volatile("s_waitcnt lgkmcnt(0)" ::: "memory");
      if (lane == 0) __hip_atomic_fetch_add((LAS unsigned*)(smem + L_FLAG + 4), 1u, __ATOMIC_RELAXED, __HIP_MEMORY_SCOPE_WORKGROUP);
    }
    if constexpr (i >= PS) {
#pragma unroll
      for (int u = npv_after(i - 1); u < npv_after(i); ++u) PV(u);
      asm volatile("" : "+v"(acc));
    }
    if constexpr (i == FIN_AT) {
      __builtin_amdgcn_sched_barrier(0);
      while (flag[1] != (unsigned)NW) __builtin_amdgcn_s_sleep(1);
      asm volatile("" ::: "memory");
      f32x4 ml[NW / 2];
#pragma unroll
      for (int w = 0; w < NW / 2; ++w) ml[w] = *(LAS f32x4*)(smem + L_ML + hA * (NW * 8) + w * 16);
      float M = fmaxf(ml[0][0], ml[0][2]);
#pragma unroll
      for (int w = 1; w < NW / 2; ++w) M = fmaxf(M, fmaxf(ml[w][0], ml[w][2]));
      float L = 0.f;
#pragma unroll
      for (int w = 0; w < NW / 2; ++w) {
        L = fmaf(ml[w][1], __builtin_amdgcn_exp2f(ml[w][0] - M), L);
        L = fmaf(ml[w][3], __builtin_amdgcn_exp2f(ml[w][2] - M), L);
      }
      f = __builtin_amdgcn_exp2f(mc - M) / L;
      asm volatile("" : "+v"(f));
    }
  });
  __builtin_amdgcn_sched_barrier(0);
#pragma unroll
  for (int u = npv_after(2 * NL - 1); u < NL; ++u) PV(u);
  *(LAS f32x4*)(smem + L_RED + sw * 1024 + lane * 16) = acc * f;
  __syncthreads();
}

extern "C" void kernel_launch(void* const* d_in, const int* in_sizes, int n_in, void* d_out, int out_size,
                              void* d_ws, size_t ws_size, hipStream_t stream) {
  fused_decode_kernel<<<256, NT, 0, stream>>>((const unsigned char*)d_in[0], (const unsigned char*)d_in[1],
                                             (const unsigned char*)d_in[2], (const unsigned char*)d_in[3],
                                             (const unsigned char*)d_in[4], (unsigned char*)d_out,
                                             (unsigned char*)d_ws);
}
```

```cpp
#include <hip/hip_runtime.h>
#include <hip/hip_bf16.h>
#include <stdint.h>
#include <utility>

#define LAS __attribute__((address_space(3)))
typedef unsigned u32x4 __attribute__((ext_vector_type(4)));
typedef unsigned u32x2 __attribute__((ext_vector_type(2)));
typedef float f32x4 __attribute__((ext_vector_type(4)));
typedef float f32x2 __attribute__((ext_vector_type(2)));
typedef short s16x4 __attribute__((ext_vector_type(4)));
typedef __bf16 bf16x8 __attribute__((ext_vector_type(8)));
typedef __bf16 bf16x2 __attribute__((ext_vector_type(2)));

namespace {

constexpr int NB = 16, DM = 576, HKV = 16, SK = 4096, DH = 128, WIN = 1024, NQ = 4096;
constexpr int NW = 8, NS = 4, NT = (NW + NS) * 64;
constexpr int NL = 256 / NW;

constexpr int KWIN = 3, VWIN = 5;
constexpr int ENDN = 16, EWIN = 7, ELEAD = 40;
constexpr int SLOT_NS = 294, TOFF = 5;
constexpr int QS = 26;
constexpr int FIN_AT = 58;
constexpr int AUX_NT = 2, AUX_SC1 = 16;
constexpr int RSRC_W3 = 0x00020000;
constexpr unsigned OOB = 0x7ffffff0u;

constexpr unsigned WS_EPOCH = 0;
constexpr unsigned WS_QG = 4096;
constexpr unsigned WS_OG = WS_QG + 256 * 1024;
constexpr unsigned WS_PG = WS_OG + 256 * 1024;
constexpr unsigned WS_BYTES = WS_PG + 36 * 16 * 2 * 1024;

constexpr int L_WQ = 0;
constexpr int L_X = L_WQ + 36864;
constexpr int L_WO = L_X + 18432;
constexpr int L_RED = L_WO + 32768;
constexpr int L_OA = L_RED + NW * 1024;
constexpr int L_Q = L_OA + 16 * 528;
constexpr int L_ML = L_Q + 512;
constexpr int L_FLAG = L_ML + NW * 16;
constexpr int L_BYTES = L_FLAG + 32;

constexpr unsigned SPIN_CAP = 1u << 18;

template <class F, int... I>
__device__ __forceinline__ void sfor_impl(F&& f, std::integer_sequence<int, I...>) { (f(std::integral_constant<int, I>{}), ...); }
template <int N, class F>
__device__ __forceinline__ void sfor(F&& f) { sfor_impl(f, std::make_integer_sequence<int, N>{}); }
constexpr int ES = QS + NL / 2, PS = ES + 4;
constexpr int npv_after(int slot) {
  int n = 0;
  for (int i = PS; i <= slot; ++i)
    for (int k = 0; k < 2; ++k)
      if (n <= i - NL - (i >= 2 * NL - ENDN ? EWIN : VWIN)) ++n;
  return n;
}

__device__ __forceinline__ float dot2(unsigned k, unsigned q, float acc) {
  return __builtin_amdgcn_fdot2_f32_bf16(__builtin_bit_cast(bf16x2, k), __builtin_bit_cast(bf16x2, q), acc, false);
}
__device__ __forceinline__ unsigned pkbf(float lo, float hi) {
  f32x2 f = {lo, hi};
  return __builtin_bit_cast(unsigned, __builtin_convertvector(f, bf16x2));
}
template <int CTRL> __device__ __forceinline__ float dpp(float x) {
  return __builtin_bit_cast(float, __builtin_amdgcn_mov_dpp(__builtin_bit_cast(int, x), CTRL, 0xf, 0xf, true));
}
constexpr int XOR1 = 0xB1, XOR2 = 0x4E, HMIRROR = 0x141, ROR8 = 0x128;
__device__ __forceinline__ float xrow_max(float x) {
  auto s = __builtin_amdgcn_permlane16_swap(__float_as_uint(x), __float_as_uint(x), false, false);
  x = fmaxf(__uint_as_float(s[0]), __uint_as_float(s[1]));
  auto t = __builtin_amdgcn_permlane32_swap(__float_as_uint(x), __float_as_uint(x), false, false);
  return fmaxf(__uint_as_float(t[0]), __uint_as_float(t[1]));
}
__device__ __forceinline__ float xrow_sum(float x) {
  auto s = __builtin_amdgcn_permlane16_swap(__float_as_uint(x), __float_as_uint(x), false, false);
  x = __uint_as_float(s[0]) + __uint_as_float(s[1]);
  auto t = __builtin_amdgcn_permlane32_swap(__float_as_uint(x), __float_as_uint(x), false, false);
  return __uint_as_float(t[0]) + __uint_as_float(t[1]);
}
__device__ __forceinline__ f32x4 mfma16(u32x4 a, u32x4 b, f32x4 c) {
  return __builtin_amdgcn_mfma_f32_16x16x32_bf16(__builtin_bit_cast(bf16x8, a), __builtin_bit_cast(bf16x8, b), c, 0, 0, 0);
}
__device__ __forceinline__ void dma16(unsigned lds, unsigned voff, __amdgpu_buffer_rsrc_t rs, unsigned soff) {
  asm volatile("s_mov_b32 m0, %0\n\ts_nop 0\n\tbuffer_load_dwordx4 %1, %2, %3 offen lds"
               :: "s"(lds), "v"(voff), "s"(rs), "s"(soff) : "memory");
}
__device__ __forceinline__ u32x2 trread(LAS unsigned char* p) {
  s16x4 v = __builtin_amdgcn_ds_read_tr16_b64_v4i16((LAS s16x4*)p);
  return __builtin_bit_cast(u32x2, v);
}
__device__ __forceinline__ unsigned wo_src(int lane) {
  const int row = ((lane >> 4) & 1) * 4 + ((lane >> 3) & 1) * 2 + (lane >> 5);
  return (unsigned)row * (DM * 2) + (unsigned)(lane & 7) * 16;
}
template <int STRIDE>
__device__ __forceinline__ void poll4(unsigned (&p)[8], __amdgpu_buffer_rsrc_t rs, unsigned off, unsigned tag) {
  unsigned cap = SPIN_CAP, t0, t1, t2;
#define PL(b) "buffer_load_dwordx4 v[" #b ":" #b "+3], %[off], %[rs], 0 offen sc1\n\t" \
              "buffer_load_dwordx4 v[" #b "+4:" #b "+7], %[off], %[rs], %[s1] offen sc1\n\t" \
              "buffer_load_dwordx4 v[" #b "+8:" #b "+11], %[off], %[rs], %[s2] offen sc1\n\t" \
              "buffer_load_dwordx4 v[" #b "+12:" #b "+15], %[off], %[rs], %[s3] offen sc1\n\t"
#define PC(b) "s_waitcnt vmcnt(4)\n\t" \
              "v_bitop3_b32 %[t0], v[" #b "+1], v[" #b "+3], %[tag] bitop3:0x7e\n\t" \
              "v_bitop3_b32 %[t1], v[" #b "+5], v[" #b "+7], %[tag] bitop3:0x7e\n\t" \
              "v_bitop3_b32 %[t2], v[" #b "+9], v[" #b "+11], %[tag] bitop3:0x7e\n\t" \
              "v_or3_b32 %[t0], %[t0], %[t1], %[t2]\n\t" \
              "v_bitop3_b32 %[t1], v[" #b "+13], v[" #b "+15], %[tag] bitop3:0x7e\n\t" \
              "v_or_b32 %[t0], %[t0], %[t1]\n\t" \
              "v_cmp_ne_u32 vcc, 0, %[t0]\n\t"
#define PM(b) "v_mov_b32 %[p0], v[" #b "]\n\tv_mov_b32 %[p1], v[" #b "+2]\n\tv_mov_b32 %[p2], v[" #b "+4]\n\tv_mov_b32 %[p3], v[" #b "+6]\n\t" \
              "v_mov_b32 %[p4], v[" #b "+8]\n\tv_mov_b32 %[p5], v[" #b "+10]\n\tv_mov_b32 %[p6], v[" #b "+12]\n\tv_mov_b32 %[p7], v[" #b "+14]\n\t"
  asm volatile(
      PL(136)
      "s_sleep %[dl]\n"
      "1:\n\t"
      PL(152)
      PC(136)
      "s_cbranch_vccz 2f\n\t"
      "s_sub_u32 %[cap], %[cap], 1\n\t"
      "s_cbranch_scc1 2f\n\t"
      PL(136)
      PC(152)
      "s_cbranch_vccnz 1b\n\t"
      PM(152)
      "s_branch 3f\n"
      "2:\n\t"
      PM(136)
      "3:\n"
      : [p0] "=&v"(p[0]), [p1] "=&v"(p[1]), [p2] "=&v"(p[2]), [p3] "=&v"(p[3]), [p4] "=&v"(p[4]), [p5] "=&v"(p[5]), [p6] "=&v"(p[6]), [p7] "=&v"(p[7]),
        [t0] "=&v"(t0), [t1] "=&v"(t1), [t2] "=&v"(t2), [cap] "+s"(cap)
      : [off] "v"(off), [rs] "s"(rs), [tag] "v"(tag), [s1] "s"(STRIDE), [s2] "s"(2 * STRIDE), [s3] "s"(3 * STRIDE), [dl] "i"(8)
      : "memory", "vcc", "scc",
        "v136", "v137", "v138", "v139", "v140", "v141", "v142", "v143", "v144", "v145", "v146", "v147", "v148", "v149", "v150", "v151",
        "v152", "v153", "v154", "v155", "v156", "v157", "v158", "v159", "v160", "v161", "v162", "v163", "v164", "v165", "v166", "v167");
#undef PL
#undef PC
#undef PM
}

}

extern "C" __global__ void __launch_bounds__(NT, (NW + NS) / 4)
fused_decode_kernel(const unsigned char* __restrict__ x, const unsigned char* __restrict__ kc,
                    const unsigned char* __restrict__ vc, const unsigned char* __restrict__ wq,
                    const unsigned char* __restrict__ wo, unsigned char* out, unsigned char* ws) {
  __shared__ __attribute__((aligned(16))) unsigned char smem_[L_BYTES];
  LAS unsigned char* smem = (LAS unsigned char*)smem_;
  const unsigned lds0 = (unsigned)(size_t)smem;
  const int tid = threadIdx.x, lane = tid & 63;
  const int wave = __builtin_amdgcn_readfirstlane(tid >> 6);
  const int bid = blockIdx.x, jj = bid >> 3, b = jj & 15, kvh = 2 * ((bid + (b >> 1)) & 7) + (jj >> 4);
  const int li = lane & 15, lg = lane >> 4;
  volatile LAS unsigned* flag = (volatile LAS unsigned*)(smem + L_FLAG);
#define PACE(n) do { __builtin_amdgcn_sched_barrier(0); asm volatile("s_waitcnt vmcnt(%0)" :: "i"((n) - 1) : "memory"); } while (0)

  if (wave >= NW) {
    const int t = wave - NW;
    const __amdgpu_buffer_rsrc_t rs_ws = __builtin_amdgcn_make_buffer_rsrc(ws, 0, WS_BYTES, RSRC_W3);
    if (t == 0) { flag[0] = 0u; flag[1] = 0u; flag[3] = 0u; flag[4] = 0u; flag[5] = 0u; }
    __builtin_amdgcn_s_setprio(3);
    if (t != 0) __syncthreads();
    if (t == 0) {
      if (b >= 8) {
        const __amdgpu_buffer_rsrc_t rs_x = __builtin_amdgcn_make_buffer_rsrc((void*)x, 0, NB * DM * 2, RSRC_W3);
        const unsigned vx = (unsigned)(lane >> 2) * (DM * 2) + (unsigned)(lane & 3) * 16;
#pragma unroll 1
        for (int s = 0; s < 18; ++s) dma16(lds0 + L_X + s * 1024, vx, rs_x, (unsigned)s * 64);
      }
      unsigned epoch = __builtin_amdgcn_raw_buffer_load_b32(rs_ws, WS_EPOCH, 0, AUX_SC1);
      __builtin_amdgcn_sched_barrier(0);
      __syncthreads();
      asm volatile("s_waitcnt vmcnt(0)" : "+v"(epoch) :: "memory");
      const unsigned tg = epoch + 1u;
      flag[2] = tg;
      if (b >= 8) {
        while (flag[3] != (unsigned)NW) __builtin_amdgcn_s_sleep(1);
        asm volatile("" ::: "memory");
        f32x4 a0 = {0.f, 0.f, 0.f, 0.f}, a1 = {0.f, 0.f, 0.f, 0.f};
        LAS unsigned char* pw = smem + L_WQ + (8 * lg + ((lane >> 2) & 3)) * 64 + (lane & 3) * 8;
        LAS unsigned char* px = smem + L_X + li * 64 + lg * 16;
#pragma unroll 6
        for (int s = 0; s < 18; ++s) {
          const u32x2 w0 = trread(pw + s * 2048), w1 = trread(pw + s * 2048 + 256);
          const u32x2 w2 = trread(pw + s * 2048 + 32), w3 = trread(pw + s * 2048 + 256 + 32);
          const u32x4 xa = *(LAS u32x4*)(px + s * 1024);
          a0 = mfma16(xa, u32x4{w0[0], w0[1], w1[0], w1[1]}, a0);
          a1 = mfma16(xa, u32x4{w2[0], w2[1], w3[0], w3[1]}, a1);
        }
        LAS unsigned* tq = (LAS unsigned*)(smem + L_OA);
#pragma unroll
        for (int r = 0; r < 4; ++r) tq[(4 * lg + r) * 16 + li] = pkbf(a0[r], a1[r]);
        asm volatile("s_waitcnt lgkmcnt(0)" ::: "memory");
        const unsigned qo = WS_QG + (unsigned)(kvh * 16 + (lane >> 3)) * 1024 + (unsigned)(b - 8) * 128 + (unsigned)(lane & 7) * 16;
#pragma unroll
        for (int k = 0; k < 2; ++k) {
          const u32x2 v = *(LAS u32x2*)(tq + k * 128 + lane * 2);
          __builtin_amdgcn_raw_buffer_store_b128(u32x4{v[0], tg, v[1], tg}, rs_ws, qo, k * 8192, AUX_SC1);
        }
        __builtin_amdgcn_s_sleep(32);
      } else {
        __builtin_amdgcn_s_sleep(96);
      }
      const unsigned qi = WS_QG + (unsigned)(kvh * 16 + b) * 1024 + (unsigned)lane * 16;
      u32x4 qr;
      for (unsigned spins = 0;; ++spins) {
        asm volatile("" ::: "memory");
        qr = __builtin_amdgcn_raw_buffer_load_b128(rs_ws, qi, 0, AUX_SC1);
        if (__builtin_amdgcn_ballot_w64(((qr[1] ^ tg) | (qr[3] ^ tg)) != 0u) == 0ull || spins > SPIN_CAP) break;
        __builtin_amdgcn_s_sleep(4);
      }
      {
        LAS unsigned* ql = (LAS unsigned*)(smem + L_Q) + 16 * (lane >> 3) + (lane & 7);
        ql[0] = __builtin_amdgcn_perm(qr[2], qr[0], 0x05040100u);
        ql[8] = __builtin_amdgcn_perm(qr[2], qr[0], 0x07060302u);
      }
      asm volatile("s_waitcnt lgkmcnt(0)" ::: "memory");
      flag[0] = 1u;
    }
    while (flag[0] == 0u) __builtin_amdgcn_s_sleep(16);
    asm volatile("" ::: "memory");
    const unsigned tag = flag[2];
    const __amdgpu_buffer_rsrc_t rs_out = __builtin_amdgcn_make_buffer_rsrc(out, 0, NB * DM * 2, RSRC_W3);
    u32x4 wf[8];
    LAS unsigned char* pwf = smem + ((lane >> 2) & 3) * 1024 + lg * 128 + ((t ^ (lane >> 2)) & 3) * 32 + (lane & 3) * 8;
#define LOADWF(dst, base) _Pragma("unroll") for (int s = 0; s < 8; ++s) { const int ro = ((s & 1) * 128 + 32 * (s >> 1)) * 128; \
        const u32x2 w0 = trread(pwf + (base) + ro), w1 = trread(pwf + (base) + ro + 512); dst[s] = u32x4{w0[0], w0[1], w1[0], w1[1]}; }
    const bool st1 = b < 8 || b == 13;
    const int cs = b < 8 ? b : 8;
    const int kv1 = 2 * (bid & 7) + (jj >> 4);
    const int rho = (b - 8) * 16 + kvh, T = rho >> 1, hf = rho & 1;
    unsigned o_pub = WS_OG + (unsigned)(kvh * 16 + b) * 1024 + (unsigned)lane * 16;
    unsigned o_get = st1 ? WS_OG + (unsigned)(kv1 * 16 + 4 * t) * 1024 + (unsigned)lane * 16
                         : WS_PG + (unsigned)((T * 16 + lg) * 2 + hf) * 1024 + (unsigned)(16 * t + li) * 16;
    unsigned o_put = st1 ? WS_PG + (unsigned)(((4 * cs + t) * 16 + kv1) * 2) * 1024 + (unsigned)lane * 16
                         : (lg == 0 ? (unsigned)(li * DM + 16 * T + 4 * t + 2 * hf) * 2 : OOB);
    asm volatile("" : "+v"(o_pub), "+v"(o_get), "+v"(o_put));
    if (st1 && t > 0) {
      const __amdgpu_buffer_rsrc_t rs_wo = __builtin_amdgcn_make_buffer_rsrc((void*)wo, 0, NQ * DM * 2, RSRC_W3);
      const unsigned vwo = wo_src(lane);
      while (flag[5] != (unsigned)NW) __builtin_amdgcn_s_sleep(2);
#pragma unroll 1
      for (int i = t - 1; i < 32; i += 3)
        dma16(lds0 + L_WO + i * 1024, vwo ^ (unsigned)((i & 3) * 32), rs_wo, (unsigned)(kv1 * 256 + 8 * i) * (DM * 2) + (unsigned)cs * 128);
    }
    __syncthreads();
    if (t == 0) {
      f32x4 pr[NW];
#pragma unroll
      for (int w = 0; w < NW; ++w) pr[w] = *(LAS f32x4*)(smem + L_RED + w * 1024 + lane * 16);
      __builtin_amdgcn_sched_barrier(0);
#pragma unroll
      for (int s = 1; s < NW; s *= 2)
#pragma unroll
        for (int w = 0; w < NW; w += 2 * s) pr[w] += pr[w + s];
      const f32x4 o = pr[0];
      __builtin_amdgcn_raw_buffer_store_b128(u32x4{pkbf(o[0], o[1]), tag, pkbf(o[2], o[3]), tag}, rs_ws, o_pub, 0, AUX_SC1);
    }
    unsigned p[8];
    if (st1) {
      if (t > 0) {
        asm volatile("s_waitcnt vmcnt(0)" ::: "memory");
        if (lane == 0) __hip_atomic_fetch_add((LAS unsigned*)(smem + L_FLAG + 16), 1u, __ATOMIC_RELAXED, __HIP_MEMORY_SCOPE_WORKGROUP);
      }
      while (flag[4] != 3u) __builtin_amdgcn_s_sleep(1);
      asm volatile("" ::: "memory");
      LOADWF(wf, L_WO)
      __builtin_amdgcn_sched_barrier(0);
      poll4<1024>(p, rs_ws, o_get, tag);
#pragma unroll
      for (int i = 0; i < 4; ++i) *(LAS u32x2*)(smem + L_OA + (4 * t + i) * 528 + lane * 8) = u32x2{p[2 * i], p[2 * i + 1]};
      __syncthreads();
      u32x4 ob[8];
#pragma unroll
      for (int s = 0; s < 8; ++s) ob[s] = *(LAS u32x4*)(smem + L_OA + li * 528 + s * 64 + lg * 16);
      __builtin_amdgcn_sched_barrier(0);
      f32x4 d = {0.f, 0.f, 0.f, 0.f};
#pragma unroll
      for (int s = 0; s < 8; ++s) d = mfma16(wf[s], ob[s], d);
      __builtin_amdgcn_raw_buffer_store_b128(u32x4{__float_as_uint(d[0]), tag, __float_as_uint(d[1]), tag}, rs_ws, o_put, 0, AUX_SC1);
      __builtin_amdgcn_raw_buffer_store_b128(u32x4{__float_as_uint(d[2]), tag, __float_as_uint(d[3]), tag}, rs_ws, o_put, 1024, AUX_SC1);
    } else if (rho < 72) {
      poll4<4 * 2048>(p, rs_ws, o_get, tag);
      float s0 = (__uint_as_float(p[0]) + __uint_as_float(p[2])) + (__uint_as_float(p[4]) + __uint_as_float(p[6]));
      float s1 = (__uint_as_float(p[1]) + __uint_as_float(p[3])) + (__uint_as_float(p[5]) + __uint_as_float(p[7]));
      s0 = xrow_sum(s0); s1 = xrow_sum(s1);
      __builtin_amdgcn_raw_buffer_store_b32(pkbf(s0, s1), rs_out, o_put, 0, 0);
      if (rho == 0 && t == 0) __builtin_amdgcn_raw_buffer_store_b32(tag, rs_ws, WS_EPOCH, 0, AUX_SC1);
    }
    asm volatile("s_waitcnt vmcnt(0)" ::: "memory");
    return;
  }

  const int sw = wave;
  const unsigned tb = (unsigned)__builtin_amdgcn_s_memrealtime();
  const bool isq = b >= 8;
  if (isq) {
    const __amdgpu_buffer_rsrc_t rs_w = __builtin_amdgcn_make_buffer_rsrc((void*)wq, 0, DM * NQ * 2, RSRC_W3);
    const unsigned vw = (unsigned)(lane >> 2) * (NQ * 2) + (unsigned)(lane & 3) * 16 + (unsigned)(kvh * 256 + 32 * (b - 8)) * 2;
    int cnt = 0;
#pragma unroll 1
    for (int i = sw; i < 36; i += NW) {
      if (cnt >= KWIN) PACE(KWIN);
      dma16(lds0 + L_WQ + i * 1024, vw, rs_w, (unsigned)i * (16 * NQ * 2));
      if (++cnt == KWIN) __syncthreads();
    }
    static_assert(KWIN <= 4, "every streaming wave must reach the barrier inside the weight loop");
  }
  const size_t kvoff = ((size_t)(b * HKV + kvh) * SK + (SK - WIN)) * (DH * 2);
  const __amdgpu_buffer_rsrc_t rs_k = __builtin_amdgcn_make_buffer_rsrc((void*)(kc + kvoff), 0, WIN * DH * 2, RSRC_W3);
  const __amdgpu_buffer_rsrc_t rs_v = __builtin_amdgcn_make_buffer_rsrc((void*)(vc + kvoff), 0, WIN * DH * 2, RSRC_W3);
  const unsigned voff = (unsigned)lane * 16 + (unsigned)sw * (NL * 1024);
  const unsigned ph = (unsigned)sw * 4096;
#define SOFF(u) (((u) * 1024 + ph) & (NL * 1024 - 1))
  u32x4 K[NL], V[NL], qA, qB;
  float y[NL], m = -INFINITY, mc = 0.f, l = 0.f, f = 0.f;
  const int hA = li >> 3;
  constexpr float C2 = 0.088388347648318440f * 1.4426950408889634f;
  const unsigned hm = (lane & 1) ? 0xffff0000u : 0x0000ffffu;
  const int rsel = (lane & 7) >> 1;
  const unsigned mk0 = rsel == 0 ? hm : 0u, mk1 = rsel == 1 ? hm : 0u, mk2 = rsel == 2 ? hm : 0u, mk3 = rsel == 3 ? hm : 0u;
  f32x4 acc = {0.f, 0.f, 0.f, 0.f};
  unsigned tnow = tb;
#define PV(u) do { const unsigned pp = pkbf(y[u], y[u]); acc = mfma16(V[u], u32x4{pp & mk0, pp & mk1, pp & mk2, pp & mk3}, acc); } while (0)
  sfor<2 * NL>([&](auto ic) __attribute__((always_inline)) {
    constexpr int i = decltype(ic)::value;
    if constexpr (i < KWIN) PACE(KWIN);
    else {
      constexpr bool endg = i >= 2 * NL - ENDN;
      PACE(endg ? EWIN : VWIN);
      if (tnow - tb < (unsigned)((i - 1 + TOFF) * SLOT_NS / 10 - (endg ? ELEAD : 0))) PACE(KWIN);
      __builtin_amdgcn_sched_barrier(0);
    }
    if constexpr (i == KWIN - 1) {
      if (isq && lane == 0) __hip_atomic_fetch_add((LAS unsigned*)(smem + L_FLAG + 12), 1u, __ATOMIC_RELAXED, __HIP_MEMORY_SCOPE_WORKGROUP);
    }
    if constexpr (i < NL) K[i] = __builtin_amdgcn_raw_buffer_load_b128(rs_k, voff, SOFF(i), AUX_NT);
    else V[i - NL] = __builtin_amdgcn_raw_buffer_load_b128(rs_v, voff, SOFF(i - NL), AUX_NT);
    __builtin_amdgcn_sched_barrier(0);
    tnow = (unsigned)__builtin_amdgcn_s_memrealtime();
    __builtin_amdgcn_sched_barrier(0);
    if constexpr (i == KWIN - 1) {
      if (!isq) __syncthreads();
    }
    if constexpr (i == 2 * NL - 1) {
      if (lane == 0) __hip_atomic_fetch_add((LAS unsigned*)(smem + L_FLAG + 20), 1u, __ATOMIC_RELAXED, __HIP_MEMORY_SCOPE_WORKGROUP);
      __builtin_amdgcn_sched_barrier(0);
    }
    if constexpr (i == QS) {
      while (flag[0] == 0u) __builtin_amdgcn_s_sleep(1);
      asm volatile("" ::: "memory");
      qA = *(LAS u32x4*)(smem + L_Q + hA * 256 + li * 16);
      qB = *(LAS u32x4*)(smem + L_Q + (hA ^ 1) * 256 + li * 16);
    }
    if constexpr (i >= QS && i < ES) {
#pragma unroll
      for (int u = 2 * (i - QS); u < 2 * (i - QS) + 2; ++u) {
        float xa = 0.f, xb = 0.f;
#pragma unroll
        for (int k = 0; k < 4; ++k) { xa = dot2(K[u][k], qA[k], xa); xb = dot2(K[u][k], qB[k], xb); }
        float t = xa + dpp<ROR8>(xb);
        t += dpp<XOR1>(t);
        t += dpp<XOR2>(t);
        t += dpp<HMIRROR>(t);
        y[u] = t;
        m = fmaxf(m, t);
        asm volatile("" : "+v"(y[u]), "+v"(m));
      }
    }
    if constexpr (i == ES) mc = xrow_max(m) * C2;
    if constexpr (i >= ES && i < PS) {
#pragma unroll
      for (int u = (NL / 4) * (i - ES); u < (NL / 4) * (i - ES + 1); ++u) { y[u] = __builtin_amdgcn_exp2f(fmaf(y[u], C2, -mc)); l += y[u]; }
      asm volatile("" : "+v"(l));
    }
    if constexpr (i == PS - 1) {
      l = xrow_sum(l);
      *(LAS f32x2*)(smem + L_ML + hA * (NW * 8) + sw * 8) = f32x2{mc, l};
      asm volatile("s_waitcnt lgkmcnt(0)" ::: "memory");
      if (lane == 0) __hip_atomic_fetch_add((LAS unsigned*)(smem + L_FLAG + 4), 1u, __ATOMIC_RELAXED, __HIP_MEMORY_SCOPE_WORKGROUP);
    }
    if constexpr (i >= PS) {
#pragma unroll
      for (int u = npv_after(i - 1); u < npv_after(i); ++u) PV(u);
      asm volatile("" : "+v"(acc));
    }
    if constexpr (i == FIN_AT) {
      __builtin_amdgcn_sched_barrier(0);
      while (flag[1] != (unsigned)NW) __builtin_amdgcn_s_sleep(1);
      asm volatile("" ::: "memory");
      f32x4 ml[NW / 2];
#pragma unroll
      for (int w = 0; w < NW / 2; ++w) ml[w] = *(LAS f32x4*)(smem + L_ML + hA * (NW * 8) + w * 16);
      float M = fmaxf(ml[0][0], ml[0][2]);
#pragma unroll
      for (int w = 1; w < NW / 2; ++w) M = fmaxf(M, fmaxf(ml[w][0], ml[w][2]));
      float L = 0.f;
#pragma unroll
      for (int w = 0; w < NW / 2; ++w) {
        L = fmaf(ml[w][1], __builtin_amdgcn_exp2f(ml[w][0] - M), L);
        L = fmaf(ml[w][3], __builtin_amdgcn_exp2f(ml[w][2] - M), L);
      }
      f = __builtin_amdgcn_exp2f(mc - M) / L;
      asm volatile("" : "+v"(f));
    }
  });
  __builtin_amdgcn_sched_barrier(0);
#pragma unroll
  for (int u = npv_after(2 * NL - 1); u < NL; ++u) PV(u);
  *(LAS f32x4*)(smem + L_RED + sw * 1024 + lane * 16) = acc * f;
  __syncthreads();
}

extern "C" void kernel_launch(void* const* d_in, const int* in_sizes, int n_in, void* d_out, int out_size,
                              void* d_ws, size_t ws_size, hipStream_t stream) {
  fused_decode_kernel<<<256, NT, 0, stream>>>((const unsigned char*)d_in[0], (const unsigned char*)d_in[1],
                                             (const unsigned char*)d_in[2], (const unsigned char*)d_in[3],
                                             (const unsigned char*)d_in[4], (unsigned char*)d_out,
                                             (unsigned char*)d_ws);
}
```

```cpp
#include <hip/hip_runtime.h>
#include <hip/hip_bf16.h>
#include <stdint.h>
#include <utility>

#define LAS __attribute__((address_space(3)))
typedef unsigned u32x4 __attribute__((ext_vector_type(4)));
typedef unsigned u32x2 __attribute__((ext_vector_type(2)));
typedef float f32x4 __attribute__((ext_vector_type(4)));
typedef float f32x2 __attribute__((ext_vector_type(2)));
typedef short s16x4 __attribute__((ext_vector_type(4)));
typedef __bf16 bf16x8 __attribute__((ext_vector_type(8)));
typedef __bf16 bf16x2 __attribute__((ext_vector_type(2)));

namespace {

constexpr int NB = 16, DM = 576, HKV = 16, SK = 4096, DH = 128, WIN = 1024, NQ = 4096;
constexpr int NW = 8, NS = 4, NT = (NW + NS) * 64;
constexpr int NL = 256 / NW;

constexpr int KWIN = 3, VWIN = 5, EWIN = 7;
constexpr int FAR = 80, ENDN = 16, ELEAD = 40;
constexpr int SLOT_NS = 294, TOFF = 5;
constexpr int QS = 26;
constexpr int FIN_AT = 58;
constexpr int AUX_NT = 2, AUX_SC1 = 16;
constexpr int RSRC_W3 = 0x00020000;
constexpr unsigned OOB = 0x7ffffff0u;

constexpr unsigned WS_EPOCH = 0;
constexpr unsigned WS_QG = 4096;
constexpr unsigned WS_OG = WS_QG + 256 * 1024;
constexpr unsigned WS_PG = WS_OG + 256 * 1024;
constexpr unsigned WS_BYTES = WS_PG + 36 * 16 * 2 * 1024;

constexpr int L_WQ = 0;
constexpr int L_X = L_WQ + 36864;
constexpr int L_WO = L_X + 18432;
constexpr int L_RED = L_WO + 32768;
constexpr int L_OA = L_RED + NW * 1024;
constexpr int L_Q = L_OA + 16 * 528;
constexpr int L_ML = L_Q + 512;
constexpr int L_FLAG = L_ML + NW * 16;
constexpr int L_BYTES = L_FLAG + 32;

constexpr unsigned SPIN_CAP = 1u << 18;

template <class F, int... I>
__device__ __forceinline__ void sfor_impl(F&& f, std::integer_sequence<int, I...>) { (f(std::integral_constant<int, I>{}), ...); }
template <int N, class F>
__device__ __forceinline__ void sfor(F&& f) { sfor_impl(f, std::make_integer_sequence<int, N>{}); }
constexpr int ES = QS + NL / 2, PS = ES + 4;
constexpr int npv_after(int slot) {
  int n = 0;
  for (int i = PS; i <= slot; ++i)
    for (int k = 0; k < 2; ++k)
      if (n <= i - NL - EWIN) ++n;
  return n;
}

__device__ __forceinline__ float dot2(unsigned k, unsigned q, float acc) {
  return __builtin_amdgcn_fdot2_f32_bf16(__builtin_bit_cast(bf16x2, k), __builtin_bit_cast(bf16x2, q), acc, false);
}
__device__ __forceinline__ unsigned pkbf(float lo, float hi) {
  f32x2 f = {lo, hi};
  return __builtin_bit_cast(unsigned, __builtin_convertvector(f, bf16x2));
}
template <int CTRL> __device__ __forceinline__ float dpp(float x) {
  return __builtin_bit_cast(float, __builtin_amdgcn_mov_dpp(__builtin_bit_cast(int, x), CTRL, 0xf, 0xf, true));
}
constexpr int XOR1 = 0xB1, XOR2 = 0x4E, HMIRROR = 0x141, ROR8 = 0x128;
__device__ __forceinline__ float xrow_max(float x) {
  auto s = __builtin_amdgcn_permlane16_swap(__float_as_uint(x), __float_as_uint(x), false, false);
  x = fmaxf(__uint_as_float(s[0]), __uint_as_float(s[1]));
  auto t = __builtin_amdgcn_permlane32_swap(__float_as_uint(x), __float_as_uint(x), false, false);
  return fmaxf(__uint_as_float(t[0]), __uint_as_float(t[1]));
}
__device__ __forceinline__ float xrow_sum(float x) {
  auto s = __builtin_amdgcn_permlane16_swap(__float_as_uint(x), __float_as_uint(x), false, false);
  x = __uint_as_float(s[0]) + __uint_as_float(s[1]);
  auto t = __builtin_amdgcn_permlane32_swap(__float_as_uint(x), __float_as_uint(x), false, false);
  return __uint_as_float(t[0]) + __uint_as_float(t[1]);
}
__device__ __forceinline__ f32x4 mfma16(u32x4 a, u32x4 b, f32x4 c) {
  return __builtin_amdgcn_mfma_f32_16x16x32_bf16(__builtin_bit_cast(bf16x8, a), __builtin_bit_cast(bf16x8, b), c, 0, 0, 0);
}
__device__ __forceinline__ void dma16(unsigned lds, unsigned voff, __amdgpu_buffer_rsrc_t rs, unsigned soff) {
  asm volatile("s_mov_b32 m0, %0\n\ts_nop 0\n\tbuffer_load_dwordx4 %1, %2, %3 offen lds"
               :: "s"(lds), "v"(voff), "s"(rs), "s"(soff) : "memory");
}
__device__ __forceinline__ u32x2 trread(LAS unsigned char* p) {
  s16x4 v = __builtin_amdgcn_ds_read_tr16_b64_v4i16((LAS s16x4*)p);
  return __builtin_bit_cast(u32x2, v);
}
__device__ __forceinline__ unsigned wo_src(int lane) {
  const int row = ((lane >> 4) & 1) * 4 + ((lane >> 3) & 1) * 2 + (lane >> 5);
  return (unsigned)row * (DM * 2) + (unsigned)(lane & 7) * 16;
}
template <int STRIDE>
__device__ __forceinline__ void poll4(unsigned (&p)[8], __amdgpu_buffer_rsrc_t rs, unsigned off, unsigned tag) {
  unsigned cap = SPIN_CAP, t0, t1, t2;
#define PL(b) "buffer_load_dwordx4 v[" #b ":" #b "+3], %[off], %[rs], 0 offen sc1\n\t" \
              "buffer_load_dwordx4 v[" #b "+4:" #b "+7], %[off], %[rs], %[s1] offen sc1\n\t" \
              "buffer_load_dwordx4 v[" #b "+8:" #b "+11], %[off], %[rs], %[s2] offen sc1\n\t" \
              "buffer_load_dwordx4 v[" #b "+12:" #b "+15], %[off], %[rs], %[s3] offen sc1\n\t"
#define PC(b) "s_waitcnt vmcnt(4)\n\t" \
              "v_bitop3_b32 %[t0], v[" #b "+1], v[" #b "+3], %[tag] bitop3:0x7e\n\t" \
              "v_bitop3_b32 %[t1], v[" #b "+5], v[" #b "+7], %[tag] bitop3:0x7e\n\t" \
              "v_bitop3_b32 %[t2], v[" #b "+9], v[" #b "+11], %[tag] bitop3:0x7e\n\t" \
              "v_or3_b32 %[t0], %[t0], %[t1], %[t2]\n\t" \
              "v_bitop3_b32 %[t1], v[" #b "+13], v[" #b "+15], %[tag] bitop3:0x7e\n\t" \
              "v_or_b32 %[t0], %[t0], %[t1]\n\t" \
              "v_cmp_ne_u32 vcc, 0, %[t0]\n\t"
#define PM(b) "v_mov_b32 %[p0], v[" #b "]\n\tv_mov_b32 %[p1], v[" #b "+2]\n\tv_mov_b32 %[p2], v[" #b "+4]\n\tv_mov_b32 %[p3], v[" #b "+6]\n\t" \
              "v_mov_b32 %[p4], v[" #b "+8]\n\tv_mov_b32 %[p5], v[" #b "+10]\n\tv_mov_b32 %[p6], v[" #b "+12]\n\tv_mov_b32 %[p7], v[" #b "+14]\n\t"
  asm volatile(
      PL(136)
      "s_sleep %[dl]\n"
      "1:\n\t"
      PL(152)
      PC(136)
      "s_cbranch_vccz 2f\n\t"
      "s_sub_u32 %[cap], %[cap], 1\n\t"
      "s_cbranch_scc1 2f\n\t"
      PL(136)
      PC(152)
      "s_cbranch_vccnz 1b\n\t"
      PM(152)
      "s_branch 3f\n"
      "2:\n\t"
      PM(136)
      "3:\n"
      : [p0] "=&v"(p[0]), [p1] "=&v"(p[1]), [p2] "=&v"(p[2]), [p3] "=&v"(p[3]), [p4] "=&v"(p[4]), [p5] "=&v"(p[5]), [p6] "=&v"(p[6]), [p7] "=&v"(p[7]),
        [t0] "=&v"(t0), [t1] "=&v"(t1), [t2] "=&v"(t2), [cap] "+s"(cap)
      : [off] "v"(off), [rs] "s"(rs), [tag] "v"(tag), [s1] "s"(STRIDE), [s2] "s"(2 * STRIDE), [s3] "s"(3 * STRIDE), [dl] "i"(8)
      : "memory", "vcc", "scc",
        "v136", "v137", "v138", "v139", "v140", "v141", "v142", "v143", "v144", "v145", "v146", "v147", "v148", "v149", "v150", "v151",
        "v152", "v153", "v154", "v155", "v156", "v157", "v158", "v159", "v160", "v161", "v162", "v163", "v164", "v165", "v166", "v167");
#undef PL
#undef PC
#undef PM
}

}

extern "C" __global__ void __launch_bounds__(NT, (NW + NS) / 4)
fused_decode_kernel(const unsigned char* __restrict__ x, const unsigned char* __restrict__ kc,
                    const unsigned char* __restrict__ vc, const unsigned char* __restrict__ wq,
                    const unsigned char* __restrict__ wo, unsigned char* out, unsigned char* ws) {
  __shared__ __attribute__((aligned(16))) unsigned char smem_[L_BYTES];
  LAS unsigned char* smem = (LAS unsigned char*)smem_;
  const unsigned lds0 = (unsigned)(size_t)smem;
  const int tid = threadIdx.x, lane = tid & 63;
  const int wave = __builtin_amdgcn_readfirstlane(tid >> 6);
  const int bid = blockIdx.x, jj = bid >> 3, b = jj & 15, kvh = 2 * ((bid + (b >> 1)) & 7) + (jj >> 4);
  const int li = lane & 15, lg = lane >> 4;
  volatile LAS unsigned* flag = (volatile LAS unsigned*)(smem + L_FLAG);
#define PACE(n) do { __builtin_amdgcn_sched_barrier(0); asm volatile("s_waitcnt vmcnt(%0)" :: "i"((n) - 1) : "memory"); } while (0)

  if (wave >= NW) {
    const int t = wave - NW;
    const __amdgpu_buffer_rsrc_t rs_ws = __builtin_amdgcn_make_buffer_rsrc(ws, 0, WS_BYTES, RSRC_W3);
    if (t == 0) { flag[0] = 0u; flag[1] = 0u; flag[3] = 0u; flag[4] = 0u; flag[5] = 0u; }
    __builtin_amdgcn_s_setprio(3);
    if (t != 0) __syncthreads();
    if (t == 0) {
      if (b >= 8) {
        const __amdgpu_buffer_rsrc_t rs_x = __builtin_amdgcn_make_buffer_rsrc((void*)x, 0, NB * DM * 2, RSRC_W3);
        const unsigned vx = (unsigned)(lane >> 2) * (DM * 2) + (unsigned)(lane & 3) * 16;
#pragma unroll 1
        for (int s = 0; s < 18; ++s) dma16(lds0 + L_X + s * 1024, vx, rs_x, (unsigned)s * 64);
      }
      unsigned epoch = __builtin_amdgcn_raw_buffer_load_b32(rs_ws, WS_EPOCH, 0, AUX_SC1);
      __builtin_amdgcn_sched_barrier(0);
      __syncthreads();
      asm volatile("s_waitcnt vmcnt(0)" : "+v"(epoch) :: "memory");
      const unsigned tg = epoch + 1u;
      flag[2] = tg;
      if (b >= 8) {
        while (flag[3] != (unsigned)NW) __builtin_amdgcn_s_sleep(1);
        asm volatile("" ::: "memory");
        f32x4 a0 = {0.f, 0.f, 0.f, 0.f}, a1 = {0.f, 0.f, 0.f, 0.f};
        LAS unsigned char* pw = smem + L_WQ + (8 * lg + ((lane >> 2) & 3)) * 64 + (lane & 3) * 8;
        LAS unsigned char* px = smem + L_X + li * 64 + lg * 16;
#pragma unroll 6
        for (int s = 0; s < 18; ++s) {
          const u32x2 w0 = trread(pw + s * 2048), w1 = trread(pw + s * 2048 + 256);
          const u32x2 w2 = trread(pw + s * 2048 + 32), w3 = trread(pw + s * 2048 + 256 + 32);
          const u32x4 xa = *(LAS u32x4*)(px + s * 1024);
          a0 = mfma16(xa, u32x4{w0[0], w0[1], w1[0], w1[1]}, a0);
          a1 = mfma16(xa, u32x4{w2[0], w2[1], w3[0], w3[1]}, a1);
        }
        LAS unsigned* tq = (LAS unsigned*)(smem + L_OA);
#pragma unroll
        for (int r = 0; r < 4; ++r) tq[(4 * lg + r) * 16 + li] = pkbf(a0[r], a1[r]);
        asm volatile("s_waitcnt lgkmcnt(0)" ::: "memory");
        const unsigned qo = WS_QG + (unsigned)(kvh * 16 + (lane >> 3)) * 1024 + (unsigned)(b - 8) * 128 + (unsigned)(lane & 7) * 16;
#pragma unroll
        for (int k = 0; k < 2; ++k) {
          const u32x2 v = *(LAS u32x2*)(tq + k * 128 + lane * 2);
          __builtin_amdgcn_raw_buffer_store_b128(u32x4{v[0], tg, v[1], tg}, rs_ws, qo, k * 8192, AUX_SC1);
        }
        __builtin_amdgcn_s_sleep(32);
      } else {
        __builtin_amdgcn_s_sleep(96);
      }
      const unsigned qi = WS_QG + (unsigned)(kvh * 16 + b) * 1024 + (unsigned)lane * 16;
      u32x4 qr;
      for (unsigned spins = 0;; ++spins) {
        asm volatile("" ::: "memory");
        qr = __builtin_amdgcn_raw_buffer_load_b128(rs_ws, qi, 0, AUX_SC1);
        if (__builtin_amdgcn_ballot_w64(((qr[1] ^ tg) | (qr[3] ^ tg)) != 0u) == 0ull || spins > SPIN_CAP) break;
        __builtin_amdgcn_s_sleep(4);
      }
      {
        LAS unsigned* ql = (LAS unsigned*)(smem + L_Q) + 16 * (lane >> 3) + (lane & 7);
        ql[0] = __builtin_amdgcn_perm(qr[2], qr[0], 0x05040100u);
        ql[8] = __builtin_amdgcn_perm(qr[2], qr[0], 0x07060302u);
      }
      asm volatile("s_waitcnt lgkmcnt(0)" ::: "memory");
      flag[0] = 1u;
    }
    while (flag[0] == 0u) __builtin_amdgcn_s_sleep(16);
    asm volatile("" ::: "memory");
    const unsigned tag = flag[2];
    const __amdgpu_buffer_rsrc_t rs_out = __builtin_amdgcn_make_buffer_rsrc(out, 0, NB * DM * 2, RSRC_W3);
    u32x4 wf[8];
    LAS unsigned char* pwf = smem + ((lane >> 2) & 3) * 1024 + lg * 128 + ((t ^ (lane >> 2)) & 3) * 32 + (lane & 3) * 8;
#define LOADWF(dst, base) _Pragma("unroll") for (int s = 0; s < 8; ++s) { const int ro = ((s & 1) * 128 + 32 * (s >> 1)) * 128; \
        const u32x2 w0 = trread(pwf + (base) + ro), w1 = trread(pwf + (base) + ro + 512); dst[s] = u32x4{w0[0], w0[1], w1[0], w1[1]}; }
    const bool st1 = b < 8 || b == 13;
    const int cs = b < 8 ? b : 8;
    const int kv1 = 2 * (bid & 7) + (jj >> 4);
    const int rho = (b - 8) * 16 + kvh, T = rho >> 1, hf = rho & 1;
    unsigned o_pub = WS_OG + (unsigned)(kvh * 16 + b) * 1024 + (unsigned)lane * 16;
    unsigned o_get = st1 ? WS_OG + (unsigned)(kv1 * 16 + 4 * t) * 1024 + (unsigned)lane * 16
                         : WS_PG + (unsigned)((T * 16 + lg) * 2 + hf) * 1024 + (unsigned)(16 * t + li) * 16;
    unsigned o_put = st1 ? WS_PG + (unsigned)(((4 * cs + t) * 16 + kv1) * 2) * 1024 + (unsigned)lane * 16
                         : (lg == 0 ? (unsigned)(li * DM + 16 * T + 4 * t + 2 * hf) * 2 : OOB);
    asm volatile("" : "+v"(o_pub), "+v"(o_get), "+v"(o_put));
    if (st1 && t > 0) {
      const __amdgpu_buffer_rsrc_t rs_wo = __builtin_amdgcn_make_buffer_rsrc((void*)wo, 0, NQ * DM * 2, RSRC_W3);
      const unsigned vwo = wo_src(lane);
      while (flag[5] != (unsigned)NW) __builtin_amdgcn_s_sleep(2);
#pragma unroll 1
      for (int i = t - 1; i < 32; i += 3)
        dma16(lds0 + L_WO + i * 1024, vwo ^ (unsigned)((i & 3) * 32), rs_wo, (unsigned)(kv1 * 256 + 8 * i) * (DM * 2) + (unsigned)cs * 128);
    }
    __syncthreads();
    if (t == 0) {
      f32x4 pr[NW];
#pragma unroll
      for (int w = 0; w < NW; ++w) pr[w] = *(LAS f32x4*)(smem + L_RED + w * 1024 + lane * 16);
      __builtin_amdgcn_sched_barrier(0);
#pragma unroll
      for (int s = 1; s < NW; s *= 2)
#pragma unroll
        for (int w = 0; w < NW; w += 2 * s) pr[w] += pr[w + s];
      const f32x4 o = pr[0];
      __builtin_amdgcn_raw_buffer_store_b128(u32x4{pkbf(o[0], o[1]), tag, pkbf(o[2], o[3]), tag}, rs_ws, o_pub, 0, AUX_SC1);
    }
    unsigned p[8];
    if (st1) {
      if (t > 0) {
        asm volatile("s_waitcnt vmcnt(0)" ::: "memory");
        if (lane == 0) __hip_atomic_fetch_add((LAS unsigned*)(smem + L_FLAG + 16), 1u, __ATOMIC_RELAXED, __HIP_MEMORY_SCOPE_WORKGROUP);
      }
      while (flag[4] != 3u) __builtin_amdgcn_s_sleep(1);
      asm volatile("" ::: "memory");
      LOADWF(wf, L_WO)
      __builtin_amdgcn_sched_barrier(0);
      poll4<1024>(p, rs_ws, o_get, tag);
#pragma unroll
      for (int i = 0; i < 4; ++i) *(LAS u32x2*)(smem + L_OA + (4 * t + i) * 528 + lane * 8) = u32x2{p[2 * i], p[2 * i + 1]};
      __syncthreads();
      u32x4 ob[8];
#pragma unroll
      for (int s = 0; s < 8; ++s) ob[s] = *(LAS u32x4*)(smem + L_OA + li * 528 + s * 64 + lg * 16);
      __builtin_amdgcn_sched_barrier(0);
      f32x4 d = {0.f, 0.f, 0.f, 0.f};
#pragma unroll
      for (int s = 0; s < 8; ++s) d = mfma16(wf[s], ob[s], d);
      __builtin_amdgcn_raw_buffer_store_b128(u32x4{__float_as_uint(d[0]), tag, __float_as_uint(d[1]), tag}, rs_ws, o_put, 0, AUX_SC1);
      __builtin_amdgcn_raw_buffer_store_b128(u32x4{__float_as_uint(d[2]), tag, __float_as_uint(d[3]), tag}, rs_ws, o_put, 1024, AUX_SC1);
    } else if (rho < 72) {
      poll4<4 * 2048>(p, rs_ws, o_get, tag);
      float s0 = (__uint_as_float(p[0]) + __uint_as_float(p[2])) + (__uint_as_float(p[4]) + __uint_as_float(p[6]));
      float s1 = (__uint_as_float(p[1]) + __uint_as_float(p[3])) + (__uint_as_float(p[5]) + __uint_as_float(p[7]));
      s0 = xrow_sum(s0); s1 = xrow_sum(s1);
      __builtin_amdgcn_raw_buffer_store_b32(pkbf(s0, s1), rs_out, o_put, 0, 0);
      if (rho == 0 && t == 0) __builtin_amdgcn_raw_buffer_store_b32(tag, rs_ws, WS_EPOCH, 0, AUX_SC1);
    }
    asm volatile("s_waitcnt vmcnt(0)" ::: "memory");
    return;
  }

  const int sw = wave;
  const unsigned tb = (unsigned)__builtin_amdgcn_s_memrealtime();
  const bool isq = b >= 8;
  if (isq) {
    const __amdgpu_buffer_rsrc_t rs_w = __builtin_amdgcn_make_buffer_rsrc((void*)wq, 0, DM * NQ * 2, RSRC_W3);
    const unsigned vw = (unsigned)(lane >> 2) * (NQ * 2) + (unsigned)(lane & 3) * 16 + (unsigned)(kvh * 256 + 32 * (b - 8)) * 2;
    int cnt = 0;
#pragma unroll 1
    for (int i = sw; i < 36; i += NW) {
      if (cnt >= KWIN) PACE(KWIN);
      dma16(lds0 + L_WQ + i * 1024, vw, rs_w, (unsigned)i * (16 * NQ * 2));
      if (++cnt == KWIN) __syncthreads();
    }
    static_assert(KWIN <= 4, "every streaming wave must reach the barrier inside the weight loop");
  }
  const size_t kvoff = ((size_t)(b * HKV + kvh) * SK + (SK - WIN)) * (DH * 2);
  const __amdgpu_buffer_rsrc_t rs_k = __builtin_amdgcn_make_buffer_rsrc((void*)(kc + kvoff), 0, WIN * DH * 2, RSRC_W3);
  const __amdgpu_buffer_rsrc_t rs_v = __builtin_amdgcn_make_buffer_rsrc((void*)(vc + kvoff), 0, WIN * DH * 2, RSRC_W3);
  const unsigned voff = (unsigned)lane * 16 + (unsigned)sw * (NL * 1024);
  const unsigned ph = (unsigned)sw * 4096;
#define SOFF(u) (((u) * 1024 + ph) & (NL * 1024 - 1))
  u32x4 K[NL], V[NL], qA, qB;
  float y[NL], m = -INFINITY, mc = 0.f, l = 0.f, f = 0.f;
  const int hA = li >> 3;
  constexpr float C2 = 0.088388347648318440f * 1.4426950408889634f;
  const unsigned hm = (lane & 1) ? 0xffff0000u : 0x0000ffffu;
  const int rsel = (lane & 7) >> 1;
  const unsigned mk0 = rsel == 0 ? hm : 0u, mk1 = rsel == 1 ? hm : 0u, mk2 = rsel == 2 ? hm : 0u, mk3 = rsel == 3 ? hm : 0u;
  f32x4 acc = {0.f, 0.f, 0.f, 0.f};
  unsigned tnow = tb;
#define PV(u) do { const unsigned pp = pkbf(y[u], y[u]); acc = mfma16(V[u], u32x4{pp & mk0, pp & mk1, pp & mk2, pp & mk3}, acc); } while (0)
  sfor<2 * NL>([&](auto ic) __attribute__((always_inline)) {
    constexpr int i = decltype(ic)::value;
    if constexpr (i < KWIN) PACE(KWIN);
    else {
      constexpr bool endg = i >= 2 * NL - ENDN;
      PACE(EWIN);
      if constexpr (!endg) { if (tnow - tb < (unsigned)((i - 1 + TOFF) * SLOT_NS / 10 + FAR)) PACE(VWIN); }
      if (tnow - tb < (unsigned)((i - 1 + TOFF) * SLOT_NS / 10 - (endg ? ELEAD : 0))) PACE(KWIN);
      __builtin_amdgcn_sched_barrier(0);
    }
    if constexpr (i == KWIN - 1) {
      if (isq && lane == 0) __hip_atomic_fetch_add((LAS unsigned*)(smem + L_FLAG + 12), 1u, __ATOMIC_RELAXED, __HIP_MEMORY_SCOPE_WORKGROUP);
    }
    if constexpr (i < NL) K[i] = __builtin_amdgcn_raw_buffer_load_b128(rs_k, voff, SOFF(i), AUX_NT);
    else V[i - NL] = __builtin_amdgcn_raw_buffer_load_b128(rs_v, voff, SOFF(i - NL), AUX_NT);
    __builtin_amdgcn_sched_barrier(0);
    tnow = (unsigned)__builtin_amdgcn_s_memrealtime();
    __builtin_amdgcn_sched_barrier(0);
    if constexpr (i == KWIN - 1) {
      if (!isq) __syncthreads();
    }
    if constexpr (i == 2 * NL - 1) {
      if (lane == 0) __hip_atomic_fetch_add((LAS unsigned*)(smem + L_FLAG + 20), 1u, __ATOMIC_RELAXED, __HIP_MEMORY_SCOPE_WORKGROUP);
      __builtin_amdgcn_sched_barrier(0);
    }
    if constexpr (i == QS) {
      while (flag[0] == 0u) __builtin_amdgcn_s_sleep(1);
      asm volatile("" ::: "memory");
      qA = *(LAS u32x4*)(smem + L_Q + hA * 256 + li * 16);
      qB = *(LAS u32x4*)(smem + L_Q + (hA ^ 1) * 256 + li * 16);
    }
    if constexpr (i >= QS && i < ES) {
#pragma unroll
      for (int u = 2 * (i - QS); u < 2 * (i - QS) + 2; ++u) {
        float xa = 0.f, xb = 0.f;
#pragma unroll
        for (int k = 0; k < 4; ++k) { xa = dot2(K[u][k], qA[k], xa); xb = dot2(K[u][k], qB[k], xb); }
        float t = xa + dpp<ROR8>(xb);
        t += dpp<XOR1>(t);
        t += dpp<XOR2>(t);
        t += dpp<HMIRROR>(t);
        y[u] = t;
        m = fmaxf(m, t);
        asm volatile("" : "+v"(y[u]), "+v"(m));
      }
    }
    if constexpr (i == ES) mc = xrow_max(m) * C2;
    if constexpr (i >= ES && i < PS) {
#pragma unroll
      for (int u = (NL / 4) * (i - ES); u < (NL / 4) * (i - ES + 1); ++u) { y[u] = __builtin_amdgcn_exp2f(fmaf(y[u], C2, -mc)); l += y[u]; }
      asm volatile("" : "+v"(l));
    }
    if constexpr (i == PS - 1) {
      l = xrow_sum(l);
      *(LAS f32x2*)(smem + L_ML + hA * (NW * 8) + sw * 8) = f32x2{mc, l};
      asm volatile("s_waitcnt lgkmcnt(0)" ::: "memory");
      if (lane == 0) __hip_atomic_fetch_add((LAS unsigned*)(smem + L_FLAG + 4), 1u, __ATOMIC_RELAXED, __HIP_MEMORY_SCOPE_WORKGROUP);
    }
    if constexpr (i >= PS) {
#pragma unroll
      for (int u = npv_after(i - 1); u < npv_after(i); ++u) PV(u);
      asm volatile("" : "+v"(acc));
    }
    if constexpr (i == FIN_AT) {
      __builtin_amdgcn_sched_barrier(0);
      while (flag[1] != (unsigned)NW) __builtin_amdgcn_s_sleep(1);
      asm volatile("" ::: "memory");
      f32x4 ml[NW / 2];
#pragma unroll
      for (int w = 0; w < NW / 2; ++w) ml[w] = *(LAS f32x4*)(smem + L_ML + hA * (NW * 8) + w * 16);
      float M = fmaxf(ml[0][0], ml[0][2]);
#pragma unroll
      for (int w = 1; w < NW / 2; ++w) M = fmaxf(M, fmaxf(ml[w][0], ml[w][2]));
      float L = 0.f;
#pragma unroll
      for (int w = 0; w < NW / 2; ++w) {
        L = fmaf(ml[w][1], __builtin_amdgcn_exp2f(ml[w][0] - M), L);
        L = fmaf(ml[w][3], __builtin_amdgcn_exp2f(ml[w][2] - M), L);
      }
      f = __builtin_amdgcn_exp2f(mc - M) / L;
      asm volatile("" : "+v"(f));
    }
  });
  __builtin_amdgcn_sched_barrier(0);
#pragma unroll
  for (int u = npv_after(2 * NL - 1); u < NL; ++u) PV(u);
  *(LAS f32x4*)(smem + L_RED + sw * 1024 + lane * 16) = acc * f;
  __syncthreads();
}

extern "C" void kernel_launch(void* const* d_in, const int* in_sizes, int n_in, void* d_out, int out_size,
                              void* d_ws, size_t ws_size, hipStream_t stream) {
  fused_decode_kernel<<<256, NT, 0, stream>>>((const unsigned char*)d_in[0], (const unsigned char*)d_in[1],
                                             (const unsigned char*)d_in[2], (const unsigned char*)d_in[3],
                                             (const unsigned char*)d_in[4], (unsigned char*)d_out,
                                             (unsigned char*)d_ws);
}
```
